```python
import torch

def _softmax(x, axis=-1):
    return torch.softmax(x, dim=axis)
def _gelu(x):
    return torch.nn.functional.gelu(x, approximate="tanh")
def _silu(x):
    return torch.nn.functional.silu(x)
def _sigmoid(x):
    return torch.sigmoid(x)
def _layernorm(x, axis=-1, eps=1e-5):
    m = x.mean(dim=axis, keepdim=True)
    v = x.var(dim=axis, keepdim=True, unbiased=False)
    return (x - m) / torch.sqrt(v + eps)
def _scatter_add(tgt, idx, src, axis=0):
    return tgt.clone().index_add_(axis, idx, src)
def _segment_sum(src, seg, n):
    out = torch.zeros((n,) + src.shape[1:], dtype=src.dtype)
    return out.index_add_(0, seg, src)
def _pad(x, pw):
    flat = tuple(v for pair in reversed(pw) for v in pair)
    return torch.nn.functional.pad(x, flat)

kernel_name = 'standalone__detection_seg_fpn_lateral__vision'

def setup_inputs():

    _w = (lambda a: (lambda t: t.to(torch.bfloat16) if t.is_floating_point() else t)(torch.as_tensor(a)))
    H = W = (224 // 2) * 2
    C = 96
    k = 3
    return {
        "c_fine": _w((lambda *s: torch.randn(*s, dtype=torch.bfloat16) * 0.05)(1, H, W, C)),
        "p_coarse": _w((lambda *s: torch.randn(*s, dtype=torch.bfloat16) * 0.05)(1, H // 2, W // 2, C)),
        "w_lat": _w((lambda *s: torch.randn(*s, dtype=torch.bfloat16) * 0.05)(C, C)),
        "w_out": _w((lambda *s: torch.randn(*s, dtype=torch.bfloat16) * 0.05)(k, k, C, C)),
    }

def reference(c_fine, p_coarse, w_lat, w_out):
    B, H, W, C = c_fine.shape
    k = 3
    lat = torch.einsum("bhwc,cd->bhwd", c_fine, w_lat)
    up = (lambda x, n, axis: torch.repeat_interleave(x, n, dim=axis))((lambda x, n, axis: torch.repeat_interleave(x, n, dim=axis))(p_coarse, 2, axis=1), 2, axis=2)
    m = lat + up
    pad = k // 2
    mp = _pad(m, ((0, 0), (pad, pad), (pad, pad), (0, 0)))
    o = None
    for di in range(k):
        for dj in range(k):
            t = torch.einsum("bhwc,cd->bhwd", mp[:, di:di + H, dj:dj + W, :], w_out[di, dj])
            o = t if o is None else o + t
    return o
```

```cpp
#include <hip/hip_runtime.h>
#include <hip/hip_bf16.h>

typedef unsigned short u16;
__device__ __forceinline__ float bf2f(u16 v){ return __uint_as_float(((unsigned)v)<<16); }
__device__ __forceinline__ u16 f2bf(float f){ __hip_bfloat16 b = __float2bfloat16(f); return *(u16*)&b; }

constexpr int H=224, W=224, C=96;

__global__ void k_m(const u16* cf, const u16* pc, const u16* wl, u16* m){
  int idx = blockIdx.x*blockDim.x+threadIdx.x;
  if(idx>=H*W*C) return;
  int d = idx%C; int pix = idx/C; int w = pix%W, h = pix/W;
  float acc=0.f;
  for(int c=0;c<C;++c) acc += bf2f(cf[pix*C+c])*bf2f(wl[c*C+d]);
  acc += bf2f(pc[((h>>1)*(W/2)+(w>>1))*C+d]);
  m[idx]=f2bf(acc);
}
__global__ void k_conv(const u16* m, const u16* wo, u16* out){
  int idx = blockIdx.x*blockDim.x+threadIdx.x;
  if(idx>=H*W*C) return;
  int d = idx%C; int pix = idx/C; int w = pix%W, h = pix/W;
  float acc=0.f;
  for(int di=0;di<3;++di)for(int dj=0;dj<3;++dj){
    int hh=h+di-1, ww=w+dj-1;
    if(hh<0||hh>=H||ww<0||ww>=W) continue;
    const u16* mp = m+(hh*W+ww)*C;
    const u16* wp = wo+(di*3+dj)*C*C+d;
    for(int c=0;c<C;++c) acc += bf2f(mp[c])*bf2f(wp[c*C]);
  }
  out[idx]=f2bf(acc);
}

extern "C" void kernel_launch(void* const* d_in, const int* in_sizes, int n_in,
                              void* d_out, int out_size, void* d_ws, size_t ws_size,
                              hipStream_t stream) {
  const u16* cf=(const u16*)d_in[0]; const u16* pc=(const u16*)d_in[1];
  const u16* wl=(const u16*)d_in[2]; const u16* wo=(const u16*)d_in[3];
  u16* m=(u16*)d_ws; u16* out=(u16*)d_out;
  int n=H*W*C;
  k_m<<<(n+255)/256,256,0,stream>>>(cf,pc,wl,m);
  k_conv<<<(n+255)/256,256,0,stream>>>(m,wo,out);
}
```

```cpp
#include <hip/hip_runtime.h>

__global__ __launch_bounds__(320, 2) __attribute__((aligned(4096)))
void fpn_kernel(const void* cf, unsigned n0, unsigned f0, const void* pc, unsigned n1, unsigned f1,
                const void* wl, unsigned n2, unsigned f2, const void* wo, unsigned n3, unsigned f3,
                void* out, unsigned n4, unsigned f4) {
  __shared__ __attribute__((aligned(16))) char lds[163840];
  unsigned ldsb = (unsigned)(size_t)lds;
  asm volatile(
    "s_load_dwordx16 s[16:31], %0, 0\n"
    "s_getpc_b64 s[14:15]\n"
    "v_lshrrev_b32 v1, 3, %2\n"
    "s_bfe_u32 s7, %1, 131075\n"
    "s_and_b32 s14, s14, 0xfffff000\n"
    "s_mul_i32 s7, s7, 5120\n"
    "v_lshl_add_u32 v1, v1, 7, s7\n"
    "global_load_dword v19, v1, s[14:15]\n"
    "v_lshrrev_b32 v1, 6, %2\n"
    "v_and_b32 v2, 63, %2\n"
    "v_readfirstlane_b32 s4, v1\n"
    "v_lshlrev_b32 v3, 4, v2\n"
    "s_bfe_u32 s7, %1, 131073\n"
    "s_lshr_b32 s8, %1, 6\n"
    "s_lshl2_add_u32 s5, s7, s8\n"
    "s_and_b32 s7, %1, 1\n"
    "s_bfe_u32 s8, %1, 196611\n"
    "s_lshl3_add_u32 s6, s7, s8\n"
    "s_cmp_lg_u32 s4, 0\n"
    "s_cbranch_scc1 LOTH\n"
    "s_setprio 3\n"
    "v_and_b32 v4, 32, v2\n"
    "v_xor_b32 v5, 32, v3\n"
    "v_xor_b32 v4, v3, v4\n"
    "v_bfrev_b32 v13, 2\n"
    "v_lshrrev_b32 v14, 2, v3\n"
    "v_add_u32 v14, 163584, v14\n"
    "ds_write_b32 v14, v13\n"
    "s_mul_i32 s7, s6, 2688\n"
    "s_sub_u32 s7, s7, 192\n"
    "v_lshlrev_b32 v14, 1, v2\n"
    "v_and_b32 v15, v14, v2\n"
    "v_and_b32 v15, 32, v15\n"
    "v_xad_u32 v8, v15, v3, s7\n"
    "v_xad_u32 v9, 32, v4, s7\n"
    "v_or_b32 v15, v14, v2\n"
    "v_and_b32 v15, 32, v15\n"
    "v_xad_u32 v10, v15, v3, s7\n"
    "s_mul_i32 s7, s6, 1344\n"
    "s_sub_u32 s7, s7, 192\n"
    "v_add_u32 v12, s7, v3\n"
    "s_cmp_eq_u32 s6, 0\n"
    "s_cselect_b32 s14, 0xfff, 0\n"
    "s_mov_b32 s15, 0\n"
    "s_cmp_eq_u32 s6, 15\n"
    "v_cndmask_b32 v8, v8, v13, s[14:15]\n"
    "v_cndmask_b32 v11, v12, v13, s[14:15]\n"
    "s_mov_b32 s14, 0\n"
    "s_cselect_b32 s15, 0xfff00000, 0\n"
    "s_mul_i32 s40, s5, 602112\n"
    "v_cndmask_b32 v10, v10, v13, s[14:15]\n"
    "s_cselect_b32 s15, -1, 0xfffff000\n"
    "s_mul_i32 s41, s5, 150528\n"
    "v_cndmask_b32 v12, v12, v13, s[14:15]\n"
    "s_sub_u32 s42, s40, 43008\n"
    "s_cselect_b32 s42, 1073741824, s42\n"
    "s_sub_u32 s43, s41, 21504\n"
    "s_cselect_b32 s43, 1073741824, s43\n"
    "s_waitcnt lgkmcnt(0)\n"
    "s_mov_b32 m0, 126976\n"
    "s_add_u32 s38, s41, 21504\n"
    "buffer_load_dwordx4 v11, s[20:23], s38 offen offset:0 lds\n"
    "buffer_load_dwordx4 v12, s[20:23], s38 offen offset:1024 lds\n"
    "s_mov_b32 m0, 82944\n"
    "s_add_u32 s38, s40, 86016\n"
    "buffer_load_dwordx4 v8, s[16:19], s38 offen offset:0 lds\n"
    "buffer_load_dwordx4 v9, s[16:19], s38 offen offset:1024 lds\n"
    "buffer_load_dwordx4 v10, s[16:19], s38 offen offset:2048 lds\n"
    "s_mov_b32 m0, 129024\n"
    "s_add_u32 s38, s41, 43008\n"
    "buffer_load_dwordx4 v11, s[20:23], s38 offen offset:0 lds\n"
    "buffer_load_dwordx4 v12, s[20:23], s38 offen offset:1024 lds\n"
    "s_mov_b32 m0, 89088\n"
    "s_add_u32 s38, s40, 172032\n"
    "buffer_load_dwordx4 v8, s[16:19], s38 offen offset:0 lds\n"
    "buffer_load_dwordx4 v9, s[16:19], s38 offen offset:1024 lds\n"
    "buffer_load_dwordx4 v10, s[16:19], s38 offen offset:2048 lds\n"
    "s_mov_b32 m0, 133120\n"
    "s_add_u32 s38, s41, 86016\n"
    "buffer_load_dwordx4 v11, s[20:23], s38 offen offset:0 lds\n"
    "buffer_load_dwordx4 v12, s[20:23], s38 offen offset:1024 lds\n"
    "s_mov_b32 m0, 104448\n"
    "s_add_u32 s38, s40, 387072\n"
    "buffer_load_dwordx4 v8, s[16:19], s38 offen offset:0 lds\n"
    "buffer_load_dwordx4 v9, s[16:19], s38 offen offset:1024 lds\n"
    "buffer_load_dwordx4 v10, s[16:19], s38 offen offset:2048 lds\n"
    "s_mov_b32 m0, 135168\n"
    "s_add_u32 s38, s41, 107520\n"
    "buffer_load_dwordx4 v11, s[20:23], s38 offen offset:0 lds\n"
    "buffer_load_dwordx4 v12, s[20:23], s38 offen offset:1024 lds\n"
    "s_mov_b32 m0, 110592\n"
    "s_add_u32 s38, s40, 473088\n"
    "buffer_load_dwordx4 v8, s[16:19], s38 offen offset:0 lds\n"
    "buffer_load_dwordx4 v9, s[16:19], s38 offen offset:1024 lds\n"
    "buffer_load_dwordx4 v10, s[16:19], s38 offen offset:2048 lds\n"
    "s_mov_b32 m0, 163460\n"
    "s_nop 0\n"
    "buffer_load_dword v13, s[24:27], 0 offen offset:0 lds\n"
    "s_mov_b32 m0, 141312\n"
    "s_mov_b32 s38, 0\n"
    "buffer_load_dwordx4 v3, s[24:27], s38 offen offset:0 lds\n"
    "buffer_load_dwordx4 v4, s[24:27], s38 offen offset:1024 lds\n"
    "buffer_load_dwordx4 v5, s[24:27], s38 offen offset:2048 lds\n"
    "s_mov_b32 m0, 144384\n"
    "s_mov_b32 s38, 3072\n"
    "buffer_load_dwordx4 v3, s[24:27], s38 offen offset:0 lds\n"
    "buffer_load_dwordx4 v4, s[24:27], s38 offen offset:1024 lds\n"
    "buffer_load_dwordx4 v5, s[24:27], s38 offen offset:2048 lds\n"
    "s_mov_b32 m0, 163460\n"
    "s_nop 0\n"
    "buffer_load_dword v13, s[24:27], 0 offen offset:4 lds\n"
    "s_mov_b32 m0, 147456\n"
    "s_mov_b32 s38, 6144\n"
    "buffer_load_dwordx4 v3, s[24:27], s38 offen offset:0 lds\n"
    "buffer_load_dwordx4 v4, s[24:27], s38 offen offset:1024 lds\n"
    "buffer_load_dwordx4 v5, s[24:27], s38 offen offset:2048 lds\n"
    "s_mov_b32 m0, 150528\n"
    "s_mov_b32 s38, 9216\n"
    "buffer_load_dwordx4 v3, s[24:27], s38 offen offset:0 lds\n"
    "buffer_load_dwordx4 v4, s[24:27], s38 offen offset:1024 lds\n"
    "buffer_load_dwordx4 v5, s[24:27], s38 offen offset:2048 lds\n"
    "s_mov_b32 m0, 163460\n"
    "s_nop 0\n"
    "buffer_load_dword v13, s[24:27], 0 offen offset:8 lds\n"
    "s_mov_b32 m0, 153600\n"
    "s_mov_b32 s38, 12288\n"
    "buffer_load_dwordx4 v3, s[24:27], s38 offen offset:0 lds\n"
    "buffer_load_dwordx4 v4, s[24:27], s38 offen offset:1024 lds\n"
    "buffer_load_dwordx4 v5, s[24:27], s38 offen offset:2048 lds\n"
    "s_mov_b32 m0, 156672\n"
    "s_mov_b32 s38, 15360\n"
    "buffer_load_dwordx4 v3, s[24:27], s38 offen offset:0 lds\n"
    "buffer_load_dwordx4 v4, s[24:27], s38 offen offset:1024 lds\n"
    "buffer_load_dwordx4 v5, s[24:27], s38 offen offset:2048 lds\n"
    "s_mov_b32 m0, 163460\n"
    "s_nop 0\n"
    "buffer_load_dword v13, s[24:27], 0 offen offset:12 lds\n"
    "s_mov_b32 m0, 86016\n"
    "s_add_u32 s38, s40, 129024\n"
    "buffer_load_dwordx4 v8, s[16:19], s38 offen offset:0 lds\n"
    "buffer_load_dwordx4 v9, s[16:19], s38 offen offset:1024 lds\n"
    "buffer_load_dwordx4 v10, s[16:19], s38 offen offset:2048 lds\n"
    "s_mov_b32 m0, 107520\n"
    "s_add_u32 s38, s40, 430080\n"
    "buffer_load_dwordx4 v8, s[16:19], s38 offen offset:0 lds\n"
    "buffer_load_dwordx4 v9, s[16:19], s38 offen offset:1024 lds\n"
    "buffer_load_dwordx4 v10, s[16:19], s38 offen offset:2048 lds\n"
    "s_mov_b32 m0, 163460\n"
    "s_nop 0\n"
    "buffer_load_dword v13, s[24:27], 0 offen offset:16 lds\n"
    "s_mov_b32 m0, 124928\n"
    "s_nop 0\n"
    "buffer_load_dwordx4 v11, s[20:23], s41 offen offset:0 lds\n"
    "buffer_load_dwordx4 v12, s[20:23], s41 offen offset:1024 lds\n"
    "s_mov_b32 m0, 79872\n"
    "s_add_u32 s38, s40, 43008\n"
    "buffer_load_dwordx4 v8, s[16:19], s38 offen offset:0 lds\n"
    "buffer_load_dwordx4 v9, s[16:19], s38 offen offset:1024 lds\n"
    "buffer_load_dwordx4 v10, s[16:19], s38 offen offset:2048 lds\n"
    "s_barrier\n"
    "s_branch LLD2\n"
    "LOTH:\n"
    "s_sub_u32 s9, s4, 1\n"
    "s_lshr_b32 s7, %1, 3\n"
    "s_lshl2_add_u32 s7, s7, s9\n"
    "s_lshl_b32 s49, s7, 12\n"
    "s_load_dwordx4 s[56:59], %0, 64\n"
    "s_waitcnt lgkmcnt(0)\n"
    "s_buffer_load_dword s51, s[24:27], s49\n"
    "s_buffer_load_dword s51, s[28:31], s49 offset:0\n"
    "s_mov_b32 s39, 400000\n"
    "s_and_b32 s10, s9, 1\n"
    "s_lshr_b32 s9, s9, 1\n"
    "s_mul_i32 s9, s9, 7\n"
    "s_lshl3_add_u32 s9, s10, s9\n"
    "s_lshl_b32 s8, s10, 1\n"
    "s_sub_u32 s8, 1, s8\n"
    "s_mul_i32 s11, s8, 3072\n"
    "v_and_b32 v28, 15, v2\n"
    "v_lshrrev_b32 v29, 4, v2\n"
    "s_mul_i32 s7, s9, 3072\n"
    "s_add_u32 s7, s7, 73728\n"
    "v_lshrrev_b32 v25, 1, v28\n"
    "v_and_b32 v26, 2, v25\n"
    "v_xor_b32 v26, v26, v29\n"
    "v_mul_u32_u24 v27, 192, v28\n"
    "v_lshl_add_u32 v26, v26, 4, v27\n"
    "v_add_u32 v10, s7, v26\n"
    "v_add_u32 v24, 1, v28\n"
    "v_lshrrev_b32 v24, 1, v24\n"
    "v_mul_u32_u24 v24, 192, v24\n"
    "v_and_or_b32 v11, v2, 48, v24\n"
    "s_lshl_b32 s50, s10, 5\n"
    "s_mul_i32 s7, s4, 3072\n"
    "s_add_u32 s7, s7, 144384\n"
    "v_lshrrev_b32 v26, 1, v29\n"
    "v_bitop3_b32 v26, v25, 3, v26 bitop3:0x6a\n"
    "v_lshlrev_b32 v27, 6, v28\n"
    "v_lshl_add_u32 v27, v26, 4, v27\n"
    "v_lshrrev_b32 v26, 1, v2\n"
    "v_and_or_b32 v27, v26, 8, v27\n"
    "v_bfrev_b32 v24, 2\n"
    "s_mov_b32 s14, 0xc000c000\n"
    "s_mov_b32 s15, s14\n"
    "v_xor_b32 v27, s50, v27\n"
    "v_cndmask_b32 v27, v27, v24, s[14:15]\n"
    "v_add_u32 v14, s7, v27\n"
    "v_xad_u32 v16, v27, 32, s7\n"
    "v_bfe_u32 v25, v2, 3, 2\n"
    "v_xor_b32 v25, v25, v2\n"
    "v_lshl_add_u32 v7, v25, 4, s7\n"
    "v_lshrrev_b32 v25, 2, v2\n"
    "s_mov_b32 s14, 0\n"
    "s_mov_b32 s15, 0xff000000\n"
    "v_lshl_add_u32 v25, v25, 7, v3\n"
    "v_cndmask_b32 v4, v25, v24, s[14:15]\n"
    "s_mul_i32 s7, s4, 6144\n"
    "s_add_u32 s7, s7, 12288\n"
    "v_lshrrev_b32 v26, 2, v28\n"
    "v_xor_b32 v26, v26, v29\n"
    "v_lshl_add_u32 v26, v28, 2, v26\n"
    "v_lshl_add_u32 v13, v26, 4, s7\n"
    "s_lshl_b32 s7, s4, 2\n"
    "s_add_u32 s7, s7, 163788\n"
    "v_lshl_add_u32 v5, v2, 20, s7\n"
    "v_bfe_u32 v27, v2, 2, 2\n"
    "v_lshl_add_u32 v28, v29, 3, v27\n"
    "v_mul_u32_u24 v28, 192, v28\n"
    "v_lshlrev_b32 v27, 1, v2\n"
    "v_and_b32 v27, 32, v27\n"
    "v_bitop3_b32 v24, v3, 48, v27 bitop3:0x6a\n"
    "s_mov_b32 s7, 141312\n"
    "v_add3_u32 v6, v28, v24, s7\n"
    "v_lshlrev_b32 v24, 3, v2\n"
    "v_and_or_b32 v24, v24, 24, v27\n"
    "v_xor_b32 v24, s50, v24\n"
    "v_add_u32 v8, v28, v24\n"
    "v_xad_u32 v9, 32, v24, v28\n"
    "v_mov_b32 v12, 163712\n"
    "s_mov_b32 s46, 0xffff0000\n"
    "s_mul_i32 s48, s8, 43008\n"
    "s_mul_i32 s47, s5, 14\n"
    "s_add_u32 s47, s47, s9\n"
    "s_sub_u32 s47, s47, 1\n"
    "s_mul_i32 s47, s47, 43008\n"
    "s_mul_i32 s7, s6, 2688\n"
    "s_add_u32 s47, s47, s7\n"
    "s_add_u32 s52, s47, s48\n"
    "s_add_u32 s53, s52, s48\n"
    "s_add_u32 s54, s53, s48\n"
    "s_add_u32 s9, s9, 121\n"
    "s_lshr_b32 s7, s9, 1\n"
    "s_lshl_b32 s40, s7, 11\n"
    "s_add_u32 s9, s9, s8\n"
    "s_lshr_b32 s7, s9, 1\n"
    "s_lshl_b32 s41, s7, 11\n"
    "s_add_u32 s9, s9, s8\n"
    "s_lshr_b32 s7, s9, 1\n"
    "s_lshl_b32 s42, s7, 11\n"
    "s_add_u32 s9, s9, s8\n"
    "s_lshr_b32 s7, s9, 1\n"
    "s_lshl_b32 s43, s7, 11\n"
    "s_add_u32 s9, s9, s8\n"
    "s_lshr_b32 s7, s9, 1\n"
    "s_lshl_b32 s44, s7, 11\n"
    "v_add_u32 v17, 0, v8\n"
    "v_add_u32 v18, 0, v9\n"
    "s_mul_i32 s7, s10, 18432\n"
    "s_sub_u32 s8, 36864, s7\n"
    "s_add_u32 s7, s7, 18432\n"
    "v_add_u32 v2, s8, v8\n"
    "v_add_u32 v3, s8, v9\n"
    "v_add_u32 v8, s7, v8\n"
    "v_add_u32 v9, s7, v9\n"
    "s_setprio 1\n"
    "s_barrier\n"
    "s_mov_b64 exec, 0\n"
    "s_cmp_eq_u32 s4, 1\n"
    "s_cbranch_scc1 LPT0\n"
    "s_cmp_eq_u32 s4, 2\n"
    "s_cbranch_scc1 LPT1\n"
    "s_cmp_eq_u32 s4, 3\n"
    "s_cbranch_scc1 LPT2\n"
    "s_branch LH0\n"
    "LLD2:\n"
    "s_mov_b32 m0, 92160\n"
    "s_add_u32 s38, s40, 215040\n"
    "buffer_load_dwordx4 v8, s[16:19], s38 offen offset:0 lds\n"
    "buffer_load_dwordx4 v9, s[16:19], s38 offen offset:1024 lds\n"
    "buffer_load_dwordx4 v10, s[16:19], s38 offen offset:2048 lds\n"
    "s_mov_b32 m0, 0\n"
    "s_mov_b32 s38, 55296\n"
    "buffer_load_dwordx4 v3, s[28:31], s38 offen offset:0 lds\n"
    "buffer_load_dwordx4 v4, s[28:31], s38 offen offset:1024 lds\n"
    "buffer_load_dwordx4 v5, s[28:31], s38 offen offset:2048 lds\n"
    "s_mov_b32 m0, 3072\n"
    "s_mov_b32 s38, 58368\n"
    "buffer_load_dwordx4 v3, s[28:31], s38 offen offset:0 lds\n"
    "buffer_load_dwordx4 v4, s[28:31], s38 offen offset:1024 lds\n"
    "buffer_load_dwordx4 v5, s[28:31], s38 offen offset:2048 lds\n"
    "s_mov_b32 m0, 101376\n"
    "s_add_u32 s38, s40, 344064\n"
    "buffer_load_dwordx4 v8, s[16:19], s38 offen offset:0 lds\n"
    "buffer_load_dwordx4 v9, s[16:19], s38 offen offset:1024 lds\n"
    "buffer_load_dwordx4 v10, s[16:19], s38 offen offset:2048 lds\n"
    "s_mov_b32 m0, 137216\n"
    "s_add_u32 s38, s41, 129024\n"
    "buffer_load_dwordx4 v11, s[20:23], s38 offen offset:0 lds\n"
    "buffer_load_dwordx4 v12, s[20:23], s38 offen offset:1024 lds\n"
    "s_mov_b32 m0, 113664\n"
    "s_add_u32 s38, s40, 516096\n"
    "buffer_load_dwordx4 v8, s[16:19], s38 offen offset:0 lds\n"
    "buffer_load_dwordx4 v9, s[16:19], s38 offen offset:1024 lds\n"
    "buffer_load_dwordx4 v10, s[16:19], s38 offen offset:2048 lds\n"
    "s_mov_b32 m0, 163460\n"
    "s_nop 0\n"
    "buffer_load_dword v13, s[24:27], 0 offen offset:20 lds\n"
    "s_mov_b32 m0, 6144\n"
    "s_mov_b32 s38, 61440\n"
    "buffer_load_dwordx4 v3, s[28:31], s38 offen offset:0 lds\n"
    "buffer_load_dwordx4 v4, s[28:31], s38 offen offset:1024 lds\n"
    "buffer_load_dwordx4 v5, s[28:31], s38 offen offset:2048 lds\n"
    "s_mov_b32 m0, 9216\n"
    "s_mov_b32 s38, 64512\n"
    "buffer_load_dwordx4 v3, s[28:31], s38 offen offset:0 lds\n"
    "buffer_load_dwordx4 v4, s[28:31], s38 offen offset:1024 lds\n"
    "buffer_load_dwordx4 v5, s[28:31], s38 offen offset:2048 lds\n"
    "s_mov_b32 m0, 163460\n"
    "s_nop 0\n"
    "buffer_load_dword v13, s[24:27], 0 offen offset:24 lds\n"
    "s_mov_b32 m0, 76800\n"
    "s_nop 0\n"
    "buffer_load_dwordx4 v8, s[16:19], s40 offen offset:0 lds\n"
    "buffer_load_dwordx4 v9, s[16:19], s40 offen offset:1024 lds\n"
    "buffer_load_dwordx4 v10, s[16:19], s40 offen offset:2048 lds\n"
    "s_mov_b32 m0, 131072\n"
    "s_add_u32 s38, s41, 64512\n"
    "buffer_load_dwordx4 v11, s[20:23], s38 offen offset:0 lds\n"
    "buffer_load_dwordx4 v12, s[20:23], s38 offen offset:1024 lds\n"
    "s_mov_b32 m0, 12288\n"
    "s_mov_b32 s38, 67584\n"
    "buffer_load_dwordx4 v3, s[28:31], s38 offen offset:0 lds\n"
    "buffer_load_dwordx4 v4, s[28:31], s38 offen offset:1024 lds\n"
    "buffer_load_dwordx4 v5, s[28:31], s38 offen offset:2048 lds\n"
    "s_mov_b32 m0, 15360\n"
    "s_mov_b32 s38, 70656\n"
    "buffer_load_dwordx4 v3, s[28:31], s38 offen offset:0 lds\n"
    "buffer_load_dwordx4 v4, s[28:31], s38 offen offset:1024 lds\n"
    "buffer_load_dwordx4 v5, s[28:31], s38 offen offset:2048 lds\n"
    "s_mov_b32 m0, 163460\n"
    "s_nop 0\n"
    "buffer_load_dword v13, s[24:27], 0 offen offset:28 lds\n"
    "s_mov_b32 m0, 95232\n"
    "s_add_u32 s38, s40, 258048\n"
    "buffer_load_dwordx4 v8, s[16:19], s38 offen offset:0 lds\n"
    "buffer_load_dwordx4 v9, s[16:19], s38 offen offset:1024 lds\n"
    "buffer_load_dwordx4 v10, s[16:19], s38 offen offset:2048 lds\n"
    "s_mov_b32 m0, 98304\n"
    "s_add_u32 s38, s40, 301056\n"
    "buffer_load_dwordx4 v8, s[16:19], s38 offen offset:0 lds\n"
    "buffer_load_dwordx4 v9, s[16:19], s38 offen offset:1024 lds\n"
    "buffer_load_dwordx4 v10, s[16:19], s38 offen offset:2048 lds\n"
    "s_mov_b32 m0, 116736\n"
    "s_add_u32 s38, s40, 559104\n"
    "buffer_load_dwordx4 v8, s[16:19], s38 offen offset:0 lds\n"
    "buffer_load_dwordx4 v9, s[16:19], s38 offen offset:1024 lds\n"
    "buffer_load_dwordx4 v10, s[16:19], s38 offen offset:2048 lds\n"
    "s_mov_b32 m0, 163460\n"
    "s_nop 0\n"
    "buffer_load_dword v13, s[24:27], 0 offen offset:32 lds\n"
    "s_mov_b32 m0, 122880\n"
    "s_nop 0\n"
    "buffer_load_dwordx4 v11, s[20:23], s43 offen offset:0 lds\n"
    "buffer_load_dwordx4 v12, s[20:23], s43 offen offset:1024 lds\n"
    "s_mov_b32 m0, 73728\n"
    "s_nop 0\n"
    "buffer_load_dwordx4 v8, s[16:19], s42 offen offset:0 lds\n"
    "buffer_load_dwordx4 v9, s[16:19], s42 offen offset:1024 lds\n"
    "buffer_load_dwordx4 v10, s[16:19], s42 offen offset:2048 lds\n"
    "s_mov_b32 m0, 139264\n"
    "s_add_u32 s38, s41, 150528\n"
    "buffer_load_dwordx4 v11, s[20:23], s38 offen offset:0 lds\n"
    "buffer_load_dwordx4 v12, s[20:23], s38 offen offset:1024 lds\n"
    "s_mov_b32 m0, 119808\n"
    "s_add_u32 s38, s40, 602112\n"
    "buffer_load_dwordx4 v8, s[16:19], s38 offen offset:0 lds\n"
    "buffer_load_dwordx4 v9, s[16:19], s38 offen offset:1024 lds\n"
    "buffer_load_dwordx4 v10, s[16:19], s38 offen offset:2048 lds\n"
    "s_mov_b32 m0, 163460\n"
    "s_nop 0\n"
    "buffer_load_dword v13, s[24:27], 0 offen offset:36 lds\n"
    "s_mov_b32 m0, 18432\n"
    "s_mov_b32 s38, 0\n"
    "buffer_load_dwordx4 v3, s[28:31], s38 offen offset:0 lds\n"
    "buffer_load_dwordx4 v4, s[28:31], s38 offen offset:1024 lds\n"
    "buffer_load_dwordx4 v5, s[28:31], s38 offen offset:2048 lds\n"
    "s_mov_b32 m0, 21504\n"
    "s_mov_b32 s38, 3072\n"
    "buffer_load_dwordx4 v3, s[28:31], s38 offen offset:0 lds\n"
    "buffer_load_dwordx4 v4, s[28:31], s38 offen offset:1024 lds\n"
    "buffer_load_dwordx4 v5, s[28:31], s38 offen offset:2048 lds\n"
    "s_mov_b32 m0, 36864\n"
    "s_mov_b32 s38, 110592\n"
    "buffer_load_dwordx4 v3, s[28:31], s38 offen offset:0 lds\n"
    "buffer_load_dwordx4 v4, s[28:31], s38 offen offset:1024 lds\n"
    "buffer_load_dwordx4 v5, s[28:31], s38 offen offset:2048 lds\n"
    "s_mov_b32 m0, 39936\n"
    "s_mov_b32 s38, 113664\n"
    "buffer_load_dwordx4 v3, s[28:31], s38 offen offset:0 lds\n"
    "buffer_load_dwordx4 v4, s[28:31], s38 offen offset:1024 lds\n"
    "buffer_load_dwordx4 v5, s[28:31], s38 offen offset:2048 lds\n"
    "s_mov_b32 m0, 163460\n"
    "s_nop 0\n"
    "buffer_load_dword v13, s[24:27], 0 offen offset:40 lds\n"
    "s_mov_b32 m0, 24576\n"
    "s_mov_b32 s38, 6144\n"
    "buffer_load_dwordx4 v3, s[28:31], s38 offen offset:0 lds\n"
    "buffer_load_dwordx4 v4, s[28:31], s38 offen offset:1024 lds\n"
    "buffer_load_dwordx4 v5, s[28:31], s38 offen offset:2048 lds\n"
    "s_mov_b32 m0, 27648\n"
    "s_mov_b32 s38, 9216\n"
    "buffer_load_dwordx4 v3, s[28:31], s38 offen offset:0 lds\n"
    "buffer_load_dwordx4 v4, s[28:31], s38 offen offset:1024 lds\n"
    "buffer_load_dwordx4 v5, s[28:31], s38 offen offset:2048 lds\n"
    "s_mov_b32 m0, 43008\n"
    "s_mov_b32 s38, 116736\n"
    "buffer_load_dwordx4 v3, s[28:31], s38 offen offset:0 lds\n"
    "buffer_load_dwordx4 v4, s[28:31], s38 offen offset:1024 lds\n"
    "buffer_load_dwordx4 v5, s[28:31], s38 offen offset:2048 lds\n"
    "s_mov_b32 m0, 46080\n"
    "s_mov_b32 s38, 119808\n"
    "buffer_load_dwordx4 v3, s[28:31], s38 offen offset:0 lds\n"
    "buffer_load_dwordx4 v4, s[28:31], s38 offen offset:1024 lds\n"
    "buffer_load_dwordx4 v5, s[28:31], s38 offen offset:2048 lds\n"
    "s_mov_b32 m0, 163460\n"
    "s_nop 0\n"
    "buffer_load_dword v13, s[24:27], 0 offen offset:44 lds\n"
    "s_mov_b32 m0, 30720\n"
    "s_mov_b32 s38, 12288\n"
    "buffer_load_dwordx4 v3, s[28:31], s38 offen offset:0 lds\n"
    "buffer_load_dwordx4 v4, s[28:31], s38 offen offset:1024 lds\n"
    "buffer_load_dwordx4 v5, s[28:31], s38 offen offset:2048 lds\n"
    "s_mov_b32 m0, 33792\n"
    "s_mov_b32 s38, 15360\n"
    "buffer_load_dwordx4 v3, s[28:31], s38 offen offset:0 lds\n"
    "buffer_load_dwordx4 v4, s[28:31], s38 offen offset:1024 lds\n"
    "buffer_load_dwordx4 v5, s[28:31], s38 offen offset:2048 lds\n"
    "s_mov_b32 m0, 49152\n"
    "s_mov_b32 s38, 122880\n"
    "buffer_load_dwordx4 v3, s[28:31], s38 offen offset:0 lds\n"
    "buffer_load_dwordx4 v4, s[28:31], s38 offen offset:1024 lds\n"
    "buffer_load_dwordx4 v5, s[28:31], s38 offen offset:2048 lds\n"
    "s_mov_b32 m0, 52224\n"
    "s_mov_b32 s38, 125952\n"
    "buffer_load_dwordx4 v3, s[28:31], s38 offen offset:0 lds\n"
    "buffer_load_dwordx4 v4, s[28:31], s38 offen offset:1024 lds\n"
    "buffer_load_dwordx4 v5, s[28:31], s38 offen offset:2048 lds\n"
    "s_mov_b32 m0, 163460\n"
    "s_nop 0\n"
    "buffer_load_dword v13, s[24:27], 0 offen offset:48 lds\n"
    "s_mov_b32 m0, 55296\n"
    "s_mov_b32 s38, 73728\n"
    "buffer_load_dwordx4 v3, s[28:31], s38 offen offset:0 lds\n"
    "buffer_load_dwordx4 v4, s[28:31], s38 offen offset:1024 lds\n"
    "buffer_load_dwordx4 v5, s[28:31], s38 offen offset:2048 lds\n"
    "s_mov_b32 m0, 58368\n"
    "s_mov_b32 s38, 76800\n"
    "buffer_load_dwordx4 v3, s[28:31], s38 offen offset:0 lds\n"
    "buffer_load_dwordx4 v4, s[28:31], s38 offen offset:1024 lds\n"
    "buffer_load_dwordx4 v5, s[28:31], s38 offen offset:2048 lds\n"
    "s_mov_b32 m0, 61440\n"
    "s_mov_b32 s38, 79872\n"
    "buffer_load_dwordx4 v3, s[28:31], s38 offen offset:0 lds\n"
    "buffer_load_dwordx4 v4, s[28:31], s38 offen offset:1024 lds\n"
    "buffer_load_dwordx4 v5, s[28:31], s38 offen offset:2048 lds\n"
    "s_mov_b32 m0, 64512\n"
    "s_mov_b32 s38, 82944\n"
    "buffer_load_dwordx4 v3, s[28:31], s38 offen offset:0 lds\n"
    "buffer_load_dwordx4 v4, s[28:31], s38 offen offset:1024 lds\n"
    "buffer_load_dwordx4 v5, s[28:31], s38 offen offset:2048 lds\n"
    "s_mov_b32 m0, 67584\n"
    "s_mov_b32 s38, 86016\n"
    "buffer_load_dwordx4 v3, s[28:31], s38 offen offset:0 lds\n"
    "buffer_load_dwordx4 v4, s[28:31], s38 offen offset:1024 lds\n"
    "buffer_load_dwordx4 v5, s[28:31], s38 offen offset:2048 lds\n"
    "s_mov_b32 m0, 70656\n"
    "s_mov_b32 s38, 89088\n"
    "buffer_load_dwordx4 v3, s[28:31], s38 offen offset:0 lds\n"
    "buffer_load_dwordx4 v4, s[28:31], s38 offen offset:1024 lds\n"
    "buffer_load_dwordx4 v5, s[28:31], s38 offen offset:2048 lds\n"
    "s_mov_b32 m0, 163460\n"
    "s_nop 0\n"
    "buffer_load_dword v13, s[24:27], 0 offen offset:52 lds\n"
    "s_lshr_b32 s7, %1, 3\n"
    "v_lshlrev_b32 v6, 2, v3\n"
    "s_lshl_b32 s7, s7, 12\n"
    "buffer_load_dword v7, v6, s[28:31], s7 offen\n"
    "s_lshr_b32 s7, %1, 3\n"
    "v_lshlrev_b32 v6, 2, v3\n"
    "s_lshl_b32 s7, s7, 12\n"
    "s_add_u32 s7, s7, 0x20000\n"
    "buffer_load_dword v7, v6, s[28:31], s7 offen\n"
    "s_barrier\n"
    "s_mov_b32 m0, 73728\n"
    "s_mov_b32 s38, 18432\n"
    "buffer_load_dwordx4 v3, s[28:31], s38 offen offset:0 lds\n"
    "buffer_load_dwordx4 v4, s[28:31], s38 offen offset:1024 lds\n"
    "buffer_load_dwordx4 v5, s[28:31], s38 offen offset:2048 lds\n"
    "s_mov_b32 m0, 76800\n"
    "s_mov_b32 s38, 21504\n"
    "buffer_load_dwordx4 v3, s[28:31], s38 offen offset:0 lds\n"
    "buffer_load_dwordx4 v4, s[28:31], s38 offen offset:1024 lds\n"
    "buffer_load_dwordx4 v5, s[28:31], s38 offen offset:2048 lds\n"
    "s_mov_b32 m0, 92160\n"
    "s_mov_b32 s38, 129024\n"
    "buffer_load_dwordx4 v3, s[28:31], s38 offen offset:0 lds\n"
    "buffer_load_dwordx4 v4, s[28:31], s38 offen offset:1024 lds\n"
    "buffer_load_dwordx4 v5, s[28:31], s38 offen offset:2048 lds\n"
    "s_mov_b32 m0, 95232\n"
    "s_mov_b32 s38, 132096\n"
    "buffer_load_dwordx4 v3, s[28:31], s38 offen offset:0 lds\n"
    "buffer_load_dwordx4 v4, s[28:31], s38 offen offset:1024 lds\n"
    "buffer_load_dwordx4 v5, s[28:31], s38 offen offset:2048 lds\n"
    "s_mov_b32 m0, 79872\n"
    "s_mov_b32 s38, 24576\n"
    "buffer_load_dwordx4 v3, s[28:31], s38 offen offset:0 lds\n"
    "buffer_load_dwordx4 v4, s[28:31], s38 offen offset:1024 lds\n"
    "buffer_load_dwordx4 v5, s[28:31], s38 offen offset:2048 lds\n"
    "s_mov_b32 m0, 82944\n"
    "s_mov_b32 s38, 27648\n"
    "buffer_load_dwordx4 v3, s[28:31], s38 offen offset:0 lds\n"
    "buffer_load_dwordx4 v4, s[28:31], s38 offen offset:1024 lds\n"
    "buffer_load_dwordx4 v5, s[28:31], s38 offen offset:2048 lds\n"
    "s_mov_b32 m0, 98304\n"
    "s_mov_b32 s38, 135168\n"
    "buffer_load_dwordx4 v3, s[28:31], s38 offen offset:0 lds\n"
    "buffer_load_dwordx4 v4, s[28:31], s38 offen offset:1024 lds\n"
    "buffer_load_dwordx4 v5, s[28:31], s38 offen offset:2048 lds\n"
    "s_mov_b32 m0, 101376\n"
    "s_mov_b32 s38, 138240\n"
    "buffer_load_dwordx4 v3, s[28:31], s38 offen offset:0 lds\n"
    "buffer_load_dwordx4 v4, s[28:31], s38 offen offset:1024 lds\n"
    "buffer_load_dwordx4 v5, s[28:31], s38 offen offset:2048 lds\n"
    "s_mov_b32 m0, 86016\n"
    "s_mov_b32 s38, 30720\n"
    "buffer_load_dwordx4 v3, s[28:31], s38 offen offset:0 lds\n"
    "buffer_load_dwordx4 v4, s[28:31], s38 offen offset:1024 lds\n"
    "buffer_load_dwordx4 v5, s[28:31], s38 offen offset:2048 lds\n"
    "s_mov_b32 m0, 89088\n"
    "s_mov_b32 s38, 33792\n"
    "buffer_load_dwordx4 v3, s[28:31], s38 offen offset:0 lds\n"
    "buffer_load_dwordx4 v4, s[28:31], s38 offen offset:1024 lds\n"
    "buffer_load_dwordx4 v5, s[28:31], s38 offen offset:2048 lds\n"
    "s_mov_b32 m0, 104448\n"
    "s_mov_b32 s38, 141312\n"
    "buffer_load_dwordx4 v3, s[28:31], s38 offen offset:0 lds\n"
    "buffer_load_dwordx4 v4, s[28:31], s38 offen offset:1024 lds\n"
    "buffer_load_dwordx4 v5, s[28:31], s38 offen offset:2048 lds\n"
    "s_mov_b32 m0, 107520\n"
    "s_mov_b32 s38, 144384\n"
    "buffer_load_dwordx4 v3, s[28:31], s38 offen offset:0 lds\n"
    "buffer_load_dwordx4 v4, s[28:31], s38 offen offset:1024 lds\n"
    "buffer_load_dwordx4 v5, s[28:31], s38 offen offset:2048 lds\n"
    "s_mov_b32 m0, 163460\n"
    "s_nop 0\n"
    "buffer_load_dword v13, s[24:27], 0 offen offset:56 lds\n"
    "s_mov_b32 m0, 110592\n"
    "s_mov_b32 s38, 92160\n"
    "buffer_load_dwordx4 v3, s[28:31], s38 offen offset:0 lds\n"
    "buffer_load_dwordx4 v4, s[28:31], s38 offen offset:1024 lds\n"
    "buffer_load_dwordx4 v5, s[28:31], s38 offen offset:2048 lds\n"
    "s_mov_b32 m0, 113664\n"
    "s_mov_b32 s38, 95232\n"
    "buffer_load_dwordx4 v3, s[28:31], s38 offen offset:0 lds\n"
    "buffer_load_dwordx4 v4, s[28:31], s38 offen offset:1024 lds\n"
    "buffer_load_dwordx4 v5, s[28:31], s38 offen offset:2048 lds\n"
    "s_mov_b32 m0, 116736\n"
    "s_mov_b32 s38, 98304\n"
    "buffer_load_dwordx4 v3, s[28:31], s38 offen offset:0 lds\n"
    "buffer_load_dwordx4 v4, s[28:31], s38 offen offset:1024 lds\n"
    "buffer_load_dwordx4 v5, s[28:31], s38 offen offset:2048 lds\n"
    "s_mov_b32 m0, 119808\n"
    "s_mov_b32 s38, 101376\n"
    "buffer_load_dwordx4 v3, s[28:31], s38 offen offset:0 lds\n"
    "buffer_load_dwordx4 v4, s[28:31], s38 offen offset:1024 lds\n"
    "buffer_load_dwordx4 v5, s[28:31], s38 offen offset:2048 lds\n"
    "s_mov_b32 m0, 122880\n"
    "s_mov_b32 s38, 104448\n"
    "buffer_load_dwordx4 v3, s[28:31], s38 offen offset:0 lds\n"
    "buffer_load_dwordx4 v4, s[28:31], s38 offen offset:1024 lds\n"
    "buffer_load_dwordx4 v5, s[28:31], s38 offen offset:2048 lds\n"
    "s_mov_b32 m0, 125952\n"
    "s_mov_b32 s38, 107520\n"
    "buffer_load_dwordx4 v3, s[28:31], s38 offen offset:0 lds\n"
    "buffer_load_dwordx4 v4, s[28:31], s38 offen offset:1024 lds\n"
    "buffer_load_dwordx4 v5, s[28:31], s38 offen offset:2048 lds\n"
    "s_mov_b32 m0, 163460\n"
    "s_nop 0\n"
    "buffer_load_dword v13, s[24:27], 0 offen offset:60 lds\n"
    "s_mov_b32 m0, 129024\n"
    "s_mov_b32 s38, 36864\n"
    "buffer_load_dwordx4 v3, s[28:31], s38 offen offset:0 lds\n"
    "buffer_load_dwordx4 v4, s[28:31], s38 offen offset:1024 lds\n"
    "buffer_load_dwordx4 v5, s[28:31], s38 offen offset:2048 lds\n"
    "s_mov_b32 m0, 132096\n"
    "s_mov_b32 s38, 39936\n"
    "buffer_load_dwordx4 v3, s[28:31], s38 offen offset:0 lds\n"
    "buffer_load_dwordx4 v4, s[28:31], s38 offen offset:1024 lds\n"
    "buffer_load_dwordx4 v5, s[28:31], s38 offen offset:2048 lds\n"
    "s_mov_b32 m0, 135168\n"
    "s_mov_b32 s38, 43008\n"
    "buffer_load_dwordx4 v3, s[28:31], s38 offen offset:0 lds\n"
    "buffer_load_dwordx4 v4, s[28:31], s38 offen offset:1024 lds\n"
    "buffer_load_dwordx4 v5, s[28:31], s38 offen offset:2048 lds\n"
    "s_mov_b32 m0, 138240\n"
    "s_mov_b32 s38, 46080\n"
    "buffer_load_dwordx4 v3, s[28:31], s38 offen offset:0 lds\n"
    "buffer_load_dwordx4 v4, s[28:31], s38 offen offset:1024 lds\n"
    "buffer_load_dwordx4 v5, s[28:31], s38 offen offset:2048 lds\n"
    "s_mov_b32 m0, 141312\n"
    "s_mov_b32 s38, 49152\n"
    "buffer_load_dwordx4 v3, s[28:31], s38 offen offset:0 lds\n"
    "buffer_load_dwordx4 v4, s[28:31], s38 offen offset:1024 lds\n"
    "buffer_load_dwordx4 v5, s[28:31], s38 offen offset:2048 lds\n"
    "s_mov_b32 m0, 144384\n"
    "s_mov_b32 s38, 52224\n"
    "buffer_load_dwordx4 v3, s[28:31], s38 offen offset:0 lds\n"
    "buffer_load_dwordx4 v4, s[28:31], s38 offen offset:1024 lds\n"
    "buffer_load_dwordx4 v5, s[28:31], s38 offen offset:2048 lds\n"
    "s_barrier\n"
    "s_mov_b32 m0, 0\n"
    "s_mov_b32 s38, 147456\n"
    "buffer_load_dwordx4 v3, s[28:31], s38 offen offset:0 lds\n"
    "buffer_load_dwordx4 v4, s[28:31], s38 offen offset:1024 lds\n"
    "buffer_load_dwordx4 v5, s[28:31], s38 offen offset:2048 lds\n"
    "s_mov_b32 m0, 3072\n"
    "s_mov_b32 s38, 150528\n"
    "buffer_load_dwordx4 v3, s[28:31], s38 offen offset:0 lds\n"
    "buffer_load_dwordx4 v4, s[28:31], s38 offen offset:1024 lds\n"
    "buffer_load_dwordx4 v5, s[28:31], s38 offen offset:2048 lds\n"
    "s_mov_b32 m0, 6144\n"
    "s_mov_b32 s38, 153600\n"
    "buffer_load_dwordx4 v3, s[28:31], s38 offen offset:0 lds\n"
    "buffer_load_dwordx4 v4, s[28:31], s38 offen offset:1024 lds\n"
    "buffer_load_dwordx4 v5, s[28:31], s38 offen offset:2048 lds\n"
    "s_mov_b32 m0, 9216\n"
    "s_mov_b32 s38, 156672\n"
    "buffer_load_dwordx4 v3, s[28:31], s38 offen offset:0 lds\n"
    "buffer_load_dwordx4 v4, s[28:31], s38 offen offset:1024 lds\n"
    "buffer_load_dwordx4 v5, s[28:31], s38 offen offset:2048 lds\n"
    "s_mov_b32 m0, 12288\n"
    "s_mov_b32 s38, 159744\n"
    "buffer_load_dwordx4 v3, s[28:31], s38 offen offset:0 lds\n"
    "buffer_load_dwordx4 v4, s[28:31], s38 offen offset:1024 lds\n"
    "buffer_load_dwordx4 v5, s[28:31], s38 offen offset:2048 lds\n"
    "s_mov_b32 m0, 15360\n"
    "s_mov_b32 s38, 162816\n"
    "buffer_load_dwordx4 v3, s[28:31], s38 offen offset:0 lds\n"
    "buffer_load_dwordx4 v4, s[28:31], s38 offen offset:1024 lds\n"
    "buffer_load_dwordx4 v5, s[28:31], s38 offen offset:2048 lds\n"
    "s_mov_b32 m0, 163460\n"
    "s_nop 0\n"
    "buffer_load_dword v13, s[24:27], 0 offen offset:64 lds\n"
    "s_load_dwordx4 s[56:59], %0, 64\n"
    "s_mul_i32 s7, s6, 2688\n"
    "s_add_u32 s40, s40, s7\n"
    "s_add_u32 s41, s40, 430080\n"
    "s_add_u32 s40, s40, 129024\n"
    "v_bfe_u32 v16, v2, 3, 2\n"
    "v_xor_b32 v16, v16, v2\n"
    "v_lshl_add_u32 v8, v16, 4, 0\n"
    "v_lshrrev_b32 v16, 2, v2\n"
    "s_mov_b32 s14, 0\n"
    "s_mov_b32 s15, 0xff000000\n"
    "v_lshl_add_u32 v16, v16, 7, v3\n"
    "v_cndmask_b32 v4, v16, v13, s[14:15]\n"
    "s_mov_b32 s39, 400000\n"
    "s_setprio 0\n"
    "v_lshlrev_b32 v9, 2, v2\n"
    "v_and_b32 v9, 12, v9\n"
    "v_add_u32 v9, 163792, v9\n"
    "v_lshrrev_b32 v16, 4, v2\n"
    "v_lshlrev_b32 v10, 1, v2\n"
    "v_bitop3_b32 v16, v10, 2, v16 bitop3:0x6a\n"
    "v_and_b32 v10, 0x3c0, v3\n"
    "v_lshl_add_u32 v10, v16, 4, v10\n"
    "v_lshlrev_b32 v16, 9, v2\n"
    "v_and_b32 v16, 1024, v16\n"
    "s_mov_b32 s7, 18432\n"
    "v_or_b32 v16, v10, v16\n"
    "v_add_u32 v10, s7, v16\n"
    "v_xad_u32 v11, v16, 16, s7\n"
    "v_xor_b32 v16, 0x400, v16\n"
    "v_add_u32 v12, s7, v16\n"
    "v_xad_u32 v14, v16, 16, s7\n"
    "s_waitcnt lgkmcnt(0)\n"
    "LE_1:\n"
    "s_sleep 2\n"
    "ds_read_b32 v15, v9 offset:0\n"
    "s_waitcnt lgkmcnt(0)\n"
    "v_cmp_eq_u32 vcc, v13, v15\n"
    "s_sub_u32 s39, s39, 1\n"
    "s_cbranch_scc1 LDEAD\n"
    "s_cbranch_vccnz LE_1\n"
    "ds_read_b128 v[24:27], v10 offset:0\n"
    "ds_read_b128 v[28:31], v12 offset:6144\n"
    "ds_read_b128 v[32:35], v11 offset:0\n"
    "ds_read_b128 v[36:39], v14 offset:6144\n"
    "ds_read_b128 v[40:43], v10 offset:12288\n"
    "ds_read_b128 v[44:47], v12 offset:18432\n"
    "ds_read_b128 v[48:51], v11 offset:12288\n"
    "ds_read_b128 v[52:55], v14 offset:18432\n"
    "s_waitcnt lgkmcnt(6)\n"
    "v_add_f32 v24, v24, v28\n"
    "v_add_f32 v25, v25, v29\n"
    "v_add_f32 v26, v26, v30\n"
    "v_add_f32 v27, v27, v31\n"
    "v_cvt_pk_bf16_f32 v24, v24, v25\n"
    "v_cvt_pk_bf16_f32 v25, v26, v27\n"
    "s_waitcnt lgkmcnt(4)\n"
    "v_add_f32 v32, v32, v36\n"
    "v_add_f32 v33, v33, v37\n"
    "v_add_f32 v34, v34, v38\n"
    "v_add_f32 v35, v35, v39\n"
    "v_cvt_pk_bf16_f32 v26, v32, v33\n"
    "v_cvt_pk_bf16_f32 v27, v34, v35\n"
    "buffer_store_dwordx4 v[24:27], v4, s[56:59], s40 offen offset:0 sc1\n"
    "s_waitcnt lgkmcnt(2)\n"
    "v_add_f32 v40, v40, v44\n"
    "v_add_f32 v41, v41, v45\n"
    "v_add_f32 v42, v42, v46\n"
    "v_add_f32 v43, v43, v47\n"
    "v_cvt_pk_bf16_f32 v40, v40, v41\n"
    "v_cvt_pk_bf16_f32 v41, v42, v43\n"
    "s_waitcnt lgkmcnt(0)\n"
    "v_add_f32 v48, v48, v52\n"
    "v_add_f32 v49, v49, v53\n"
    "v_add_f32 v50, v50, v54\n"
    "v_add_f32 v51, v51, v55\n"
    "v_cvt_pk_bf16_f32 v42, v48, v49\n"
    "v_cvt_pk_bf16_f32 v43, v50, v51\n"
    "buffer_store_dwordx4 v[40:43], v4, s[56:59], s41 offen offset:0 sc1\n"
    "LE_2:\n"
    "s_sleep 2\n"
    "ds_read_b32 v15, v9 offset:16\n"
    "s_waitcnt lgkmcnt(0)\n"
    "v_cmp_eq_u32 vcc, v13, v15\n"
    "s_sub_u32 s39, s39, 1\n"
    "s_cbranch_scc1 LDEAD\n"
    "s_cbranch_vccnz LE_2\n"
    "ds_read_b128 v[56:59], v10 offset:2048\n"
    "ds_read_b128 v[60:63], v12 offset:8192\n"
    "ds_read_b128 v[64:67], v11 offset:2048\n"
    "ds_read_b128 v[68:71], v14 offset:8192\n"
    "ds_read_b128 v[72:75], v10 offset:14336\n"
    "ds_read_b128 v[76:79], v12 offset:20480\n"
    "ds_read_b128 v[80:83], v11 offset:14336\n"
    "ds_read_b128 v[84:87], v14 offset:20480\n"
    "s_waitcnt lgkmcnt(6)\n"
    "v_add_f32 v56, v56, v60\n"
    "v_add_f32 v57, v57, v61\n"
    "v_add_f32 v58, v58, v62\n"
    "v_add_f32 v59, v59, v63\n"
    "v_cvt_pk_bf16_f32 v56, v56, v57\n"
    "v_cvt_pk_bf16_f32 v57, v58, v59\n"
    "s_waitcnt lgkmcnt(4)\n"
    "v_add_f32 v64, v64, v68\n"
    "v_add_f32 v65, v65, v69\n"
    "v_add_f32 v66, v66, v70\n"
    "v_add_f32 v67, v67, v71\n"
    "v_cvt_pk_bf16_f32 v58, v64, v65\n"
    "v_cvt_pk_bf16_f32 v59, v66, v67\n"
    "buffer_store_dwordx4 v[56:59], v4, s[56:59], s40 offen offset:64 sc1\n"
    "s_waitcnt lgkmcnt(2)\n"
    "v_add_f32 v72, v72, v76\n"
    "v_add_f32 v73, v73, v77\n"
    "v_add_f32 v74, v74, v78\n"
    "v_add_f32 v75, v75, v79\n"
    "v_cvt_pk_bf16_f32 v72, v72, v73\n"
    "v_cvt_pk_bf16_f32 v73, v74, v75\n"
    "s_waitcnt lgkmcnt(0)\n"
    "v_add_f32 v80, v80, v84\n"
    "v_add_f32 v81, v81, v85\n"
    "v_add_f32 v82, v82, v86\n"
    "v_add_f32 v83, v83, v87\n"
    "v_cvt_pk_bf16_f32 v74, v80, v81\n"
    "v_cvt_pk_bf16_f32 v75, v82, v83\n"
    "buffer_store_dwordx4 v[72:75], v4, s[56:59], s41 offen offset:64 sc1\n"
    "LE_3:\n"
    "s_sleep 2\n"
    "ds_read_b32 v15, v9 offset:32\n"
    "s_waitcnt lgkmcnt(0)\n"
    "v_cmp_eq_u32 vcc, v13, v15\n"
    "s_sub_u32 s39, s39, 1\n"
    "s_cbranch_scc1 LDEAD\n"
    "s_cbranch_vccnz LE_3\n"
    "ds_read_b128 v[88:91], v10 offset:4096\n"
    "ds_read_b128 v[92:95], v12 offset:10240\n"
    "ds_read_b128 v[96:99], v11 offset:4096\n"
    "ds_read_b128 v[100:103], v14 offset:10240\n"
    "ds_read_b128 v[104:107], v10 offset:16384\n"
    "ds_read_b128 v[108:111], v12 offset:22528\n"
    "ds_read_b128 v[112:115], v11 offset:16384\n"
    "ds_read_b128 v[116:119], v14 offset:22528\n"
    "s_waitcnt lgkmcnt(6)\n"
    "v_add_f32 v88, v88, v92\n"
    "v_add_f32 v89, v89, v93\n"
    "v_add_f32 v90, v90, v94\n"
    "v_add_f32 v91, v91, v95\n"
    "v_cvt_pk_bf16_f32 v88, v88, v89\n"
    "v_cvt_pk_bf16_f32 v89, v90, v91\n"
    "s_waitcnt lgkmcnt(4)\n"
    "v_add_f32 v96, v96, v100\n"
    "v_add_f32 v97, v97, v101\n"
    "v_add_f32 v98, v98, v102\n"
    "v_add_f32 v99, v99, v103\n"
    "v_cvt_pk_bf16_f32 v90, v96, v97\n"
    "v_cvt_pk_bf16_f32 v91, v98, v99\n"
    "buffer_store_dwordx4 v[88:91], v4, s[56:59], s40 offen offset:128 sc1\n"
    "s_waitcnt lgkmcnt(2)\n"
    "v_add_f32 v104, v104, v108\n"
    "v_add_f32 v105, v105, v109\n"
    "v_add_f32 v106, v106, v110\n"
    "v_add_f32 v107, v107, v111\n"
    "v_cvt_pk_bf16_f32 v104, v104, v105\n"
    "v_cvt_pk_bf16_f32 v105, v106, v107\n"
    "s_waitcnt lgkmcnt(0)\n"
    "v_add_f32 v112, v112, v116\n"
    "v_add_f32 v113, v113, v117\n"
    "v_add_f32 v114, v114, v118\n"
    "v_add_f32 v115, v115, v119\n"
    "v_cvt_pk_bf16_f32 v106, v112, v113\n"
    "v_cvt_pk_bf16_f32 v107, v114, v115\n"
    "buffer_store_dwordx4 v[104:107], v4, s[56:59], s41 offen offset:128 sc1\n"
    "s_endpgm\n"
    "LCS:\n"
    "s_mov_b64 exec, -1\n"
    "ds_read_b32 v15, v12 offset:0\n"
    "v_add_u32 v21, s43, v11\n"
    "v_mad_i32_i24 v20, s11, 3, v10\n"
    "v_add_u32 v25, s44, v11\n"
    "v_mad_i32_i24 v24, s11, 4, v10\n"
    "s_waitcnt lgkmcnt(0)\n"
    "v_cmp_ne_u32 vcc, 0, v15\n"
    "s_nop 0\n"
    "s_cbranch_vccz LG_4\n"
    "LP_5:\n"
    "ds_read_b32 v15, v12 offset:0\n"
    "s_waitcnt lgkmcnt(0)\n"
    "v_cmp_ne_u32 vcc, 0, v15\n"
    "s_sub_u32 s39, s39, 1\n"
    "s_cbranch_scc1 LDEAD\n"
    "s_cbranch_vccnz LP_5\n"
    "LG_4:\n"
    "ds_read_b128 v[28:31], v21 offset:0\n"
    "ds_read_b128 v[32:35], v20 offset:0\n"
    "ds_read_b128 v[36:39], v21 offset:64\n"
    "ds_read_b128 v[40:43], v21 offset:128\n"
    "ds_read_b128 v[44:47], v20 offset:64\n"
    "ds_read_b128 v[48:51], v20 offset:128\n"
    "ds_read_b32 v1, v12 offset:4\n"
    "s_waitcnt lgkmcnt(6)\n"
    "v_and_b32 v55, s46, v29\n"
    "v_lshlrev_b32 v54, 16, v29\n"
    "v_and_b32 v53, s46, v28\n"
    "v_lshlrev_b32 v52, 16, v28\n"
    "v_and_b32 v59, s46, v31\n"
    "v_lshlrev_b32 v58, 16, v31\n"
    "v_and_b32 v57, s46, v30\n"
    "v_lshlrev_b32 v56, 16, v30\n"
    "s_waitcnt lgkmcnt(4)\n"
    "v_and_b32 v63, s46, v37\n"
    "v_lshlrev_b32 v62, 16, v37\n"
    "v_and_b32 v61, s46, v36\n"
    "v_lshlrev_b32 v60, 16, v36\n"
    "v_and_b32 v67, s46, v39\n"
    "v_lshlrev_b32 v66, 16, v39\n"
    "s_waitcnt lgkmcnt(0)\n"
    "v_cmp_ne_u32 vcc, 0, v1\n"
    "v_and_b32 v65, s46, v38\n"
    "s_cbranch_vccz LG_6\n"
    "LP_7:\n"
    "ds_read_b32 v1, v12 offset:4\n"
    "s_waitcnt lgkmcnt(0)\n"
    "v_cmp_ne_u32 vcc, 0, v1\n"
    "s_sub_u32 s39, s39, 1\n"
    "s_cbranch_scc1 LDEAD\n"
    "s_cbranch_vccnz LP_7\n"
    "LG_6:\n"
    "ds_read_b64_tr_b16 v[70:71], v6 offset:768\n"
    "ds_read_b64_tr_b16 v[68:69], v6 offset:0\n"
    "v_lshlrev_b32 v64, 16, v38\n"
    "v_and_b32 v75, s46, v41\n"
    "ds_read_b64_tr_b16 v[78:79], v6 offset:776\n"
    "ds_read_b64_tr_b16 v[76:77], v6 offset:8\n"
    "v_lshlrev_b32 v74, 16, v41\n"
    "v_and_b32 v73, s46, v40\n"
    "ds_read_b64_tr_b16 v[82:83], v6 offset:832\n"
    "ds_read_b64_tr_b16 v[80:81], v6 offset:64\n"
    "v_lshlrev_b32 v72, 16, v40\n"
    "v_and_b32 v87, s46, v43\n"
    "ds_read_b32 v15, v12 offset:8\n"
    "ds_read_b64_tr_b16 v[90:91], v6 offset:840\n"
    "v_lshlrev_b32 v86, 16, v43\n"
    "v_and_b32 v85, s46, v42\n"
    "ds_read_b64_tr_b16 v[88:89], v6 offset:72\n"
    "ds_read_b64_tr_b16 v[94:95], v6 offset:896\n"
    "v_lshlrev_b32 v84, 16, v42\n"
    "ds_read_b64_tr_b16 v[92:93], v6 offset:128\n"
    "ds_read_b64_tr_b16 v[98:99], v6 offset:904\n"
    "s_waitcnt lgkmcnt(10)\n"
    "v_mfma_f32_16x16x32_bf16 v[52:55], v[68:71], v[32:35], v[52:55]\n"
    "ds_read_b64_tr_b16 v[96:97], v6 offset:136\n"
    "s_waitcnt lgkmcnt(9)\n"
    "v_mfma_f32_16x16x32_bf16 v[56:59], v[76:79], v[32:35], v[56:59]\n"
    "s_waitcnt lgkmcnt(7)\n"
    "v_mfma_f32_16x16x32_bf16 v[60:63], v[80:83], v[32:35], v[60:63]\n"
    "s_waitcnt lgkmcnt(6)\n"
    "v_cmp_ne_u32 vcc, 0, v15\n"
    "s_waitcnt lgkmcnt(4)\n"
    "v_mfma_f32_16x16x32_bf16 v[64:67], v[88:91], v[32:35], v[64:67]\n"
    "ds_read_b32 v1, v12 offset:12\n"
    "s_cbranch_vccz LG_8\n"
    "LP_9:\n"
    "ds_read_b32 v15, v12 offset:8\n"
    "s_waitcnt lgkmcnt(0)\n"
    "v_cmp_ne_u32 vcc, 0, v15\n"
    "s_sub_u32 s39, s39, 1\n"
    "s_cbranch_scc1 LDEAD\n"
    "s_cbranch_vccnz LP_9\n"
    "LG_8:\n"
    "s_waitcnt lgkmcnt(3)\n"
    "v_mfma_f32_16x16x32_bf16 v[72:75], v[92:95], v[32:35], v[72:75]\n"
    "ds_read_b64_tr_b16 v[102:103], v6 offset:6912\n"
    "s_waitcnt lgkmcnt(2)\n"
    "v_mfma_f32_16x16x32_bf16 v[84:87], v[96:99], v[32:35], v[84:87]\n"
    "ds_read_b64_tr_b16 v[100:101], v6 offset:6144\n"
    "ds_read_b64_tr_b16 v[106:107], v6 offset:6920\n"
    "ds_read_b64_tr_b16 v[104:105], v6 offset:6152\n"
    "ds_read_b64_tr_b16 v[110:111], v6 offset:6976\n"
    "ds_read_b64_tr_b16 v[108:109], v6 offset:6208\n"
    "ds_read_b64_tr_b16 v[114:115], v6 offset:6984\n"
    "ds_read_b64_tr_b16 v[112:113], v6 offset:6216\n"
    "ds_read_b64_tr_b16 v[118:119], v6 offset:7040\n"
    "ds_read_b64_tr_b16 v[116:117], v6 offset:6272\n"
    "ds_read_b64_tr_b16 v[122:123], v6 offset:7048\n"
    "s_waitcnt lgkmcnt(11)\n"
    "v_cmp_ne_u32 vcc, 0, v1\n"
    "ds_read_b64_tr_b16 v[120:121], v6 offset:6280\n"
    "s_cbranch_vccz LG_10\n"
    "LP_11:\n"
    "ds_read_b32 v1, v12 offset:12\n"
    "s_waitcnt lgkmcnt(0)\n"
    "v_cmp_ne_u32 vcc, 0, v1\n"
    "s_sub_u32 s39, s39, 1\n"
    "s_cbranch_scc1 LDEAD\n"
    "s_cbranch_vccnz LP_11\n"
    "LG_10:\n"
    "s_waitcnt lgkmcnt(10)\n"
    "v_mfma_f32_16x16x32_bf16 v[52:55], v[100:103], v[44:47], v[52:55]\n"
    "ds_read_b64_tr_b16 v[126:127], v6 offset:13056\n"
    "s_waitcnt lgkmcnt(9)\n"
    "v_mfma_f32_16x16x32_bf16 v[56:59], v[104:107], v[44:47], v[56:59]\n"
    "ds_read_b64_tr_b16 v[124:125], v6 offset:12288\n"
    "s_waitcnt lgkmcnt(8)\n"
    "v_mfma_f32_16x16x32_bf16 v[60:63], v[108:111], v[44:47], v[60:63]\n"
    "ds_read_b64_tr_b16 v[130:131], v6 offset:13064\n"
    "s_waitcnt lgkmcnt(7)\n"
    "v_mfma_f32_16x16x32_bf16 v[64:67], v[112:115], v[44:47], v[64:67]\n"
    "ds_read_b64_tr_b16 v[128:129], v6 offset:12296\n"
    "s_waitcnt lgkmcnt(6)\n"
    "v_mfma_f32_16x16x32_bf16 v[72:75], v[116:119], v[44:47], v[72:75]\n"
    "ds_read_b64_tr_b16 v[134:135], v6 offset:13120\n"
    "ds_read_b64_tr_b16 v[132:133], v6 offset:12352\n"
    "s_waitcnt lgkmcnt(6)\n"
    "v_mfma_f32_16x16x32_bf16 v[84:87], v[120:123], v[44:47], v[84:87]\n"
    "ds_read_b64_tr_b16 v[138:139], v6 offset:13128\n"
    "ds_read_b32 v15, v12 offset:16\n"
    "ds_read_b64_tr_b16 v[136:137], v6 offset:12360\n"
    "ds_read_b64_tr_b16 v[142:143], v6 offset:13184\n"
    "ds_read_b64_tr_b16 v[140:141], v6 offset:12416\n"
    "s_waitcnt lgkmcnt(9)\n"
    "v_mfma_f32_16x16x32_bf16 v[52:55], v[124:127], v[48:51], v[52:55]\n"
    "ds_read_b64_tr_b16 v[146:147], v6 offset:13192\n"
    "ds_read_b64_tr_b16 v[144:145], v6 offset:12424\n"
    "s_waitcnt lgkmcnt(9)\n"
    "v_mfma_f32_16x16x32_bf16 v[56:59], v[128:131], v[48:51], v[56:59]\n"
    "s_waitcnt lgkmcnt(7)\n"
    "v_mfma_f32_16x16x32_bf16 v[60:63], v[132:135], v[48:51], v[60:63]\n"
    "s_nop 1\n"
    "v_cvt_pk_bf16_f32 v149, v54, v55\n"
    "v_cvt_pk_bf16_f32 v148, v52, v53\n"
    "s_waitcnt lgkmcnt(4)\n"
    "v_mfma_f32_16x16x32_bf16 v[64:67], v[136:139], v[48:51], v[64:67]\n"
    "v_cmp_ne_u32 vcc, 0, v15\n"
    "s_waitcnt lgkmcnt(2)\n"
    "v_mfma_f32_16x16x32_bf16 v[72:75], v[140:143], v[48:51], v[72:75]\n"
    "v_cvt_pk_bf16_f32 v151, v58, v59\n"
    "v_cvt_pk_bf16_f32 v150, v56, v57\n"
    "v_cvt_pk_bf16_f32 v153, v62, v63\n"
    "s_cbranch_vccz LG_12\n"
    "LP_13:\n"
    "ds_read_b32 v15, v12 offset:16\n"
    "s_waitcnt lgkmcnt(0)\n"
    "v_cmp_ne_u32 vcc, 0, v15\n"
    "s_sub_u32 s39, s39, 1\n"
    "s_cbranch_scc1 LDEAD\n"
    "s_cbranch_vccnz LP_13\n"
    "LG_12:\n"
    "s_waitcnt lgkmcnt(0)\n"
    "v_mfma_f32_16x16x32_bf16 v[84:87], v[144:147], v[48:51], v[84:87]\n"
    "ds_read_b128 v[156:159], v25 offset:0\n"
    "ds_read_b128 v[160:163], v24 offset:0\n"
    "ds_read_b128 v[164:167], v25 offset:64\n"
    "ds_read_b128 v[168:171], v24 offset:64\n"
    "ds_read_b128 v[172:175], v24 offset:128\n"
    "ds_read_b128 v[176:179], v25 offset:128\n"
    "v_add_u32 v181, s42, v11\n"
    "v_mad_i32_i24 v180, s11, 2, v10\n"
    "v_cvt_pk_bf16_f32 v152, v60, v61\n"
    "v_cvt_pk_bf16_f32 v155, v66, v67\n"
    "v_cvt_pk_bf16_f32 v154, v64, v65\n"
    "v_cvt_pk_bf16_f32 v185, v74, v75\n"
    "v_cvt_pk_bf16_f32 v184, v72, v73\n"
    "v_cvt_pk_bf16_f32 v187, v86, v87\n"
    "v_cvt_pk_bf16_f32 v186, v84, v85\n"
    "s_waitcnt lgkmcnt(5)\n"
    "v_and_b32 v191, s46, v157\n"
    "v_lshlrev_b32 v190, 16, v157\n"
    "v_and_b32 v189, s46, v156\n"
    "v_lshlrev_b32 v188, 16, v156\n"
    "v_and_b32 v195, s46, v159\n"
    "s_waitcnt lgkmcnt(4)\n"
    "v_mfma_f32_16x16x32_bf16 v[188:191], v[68:71], v[160:163], v[188:191]\n"
    "v_lshlrev_b32 v194, 16, v159\n"
    "s_waitcnt lgkmcnt(2)\n"
    "v_mfma_f32_16x16x32_bf16 v[188:191], v[100:103], v[168:171], v[188:191]\n"
    "v_and_b32 v193, s46, v158\n"
    "s_waitcnt lgkmcnt(1)\n"
    "v_mfma_f32_16x16x32_bf16 v[188:191], v[124:127], v[172:175], v[188:191]\n"
    "v_lshlrev_b32 v192, 16, v158\n"
    "v_and_b32 v199, s46, v165\n"
    "v_lshlrev_b32 v198, 16, v165\n"
    "v_mfma_f32_16x16x32_bf16 v[192:195], v[76:79], v[160:163], v[192:195]\n"
    "v_and_b32 v197, s46, v164\n"
    "v_lshlrev_b32 v196, 16, v164\n"
    "v_mfma_f32_16x16x32_bf16 v[192:195], v[104:107], v[168:171], v[192:195]\n"
    "v_and_b32 v203, s46, v167\n"
    "v_lshlrev_b32 v202, 16, v167\n"
    "v_mfma_f32_16x16x32_bf16 v[192:195], v[128:131], v[172:175], v[192:195]\n"
    "v_and_b32 v201, s46, v166\n"
    "v_lshlrev_b32 v200, 16, v166\n"
    "v_mfma_f32_16x16x32_bf16 v[196:199], v[80:83], v[160:163], v[196:199]\n"
    "s_waitcnt lgkmcnt(0)\n"
    "v_and_b32 v207, s46, v177\n"
    "v_mfma_f32_16x16x32_bf16 v[196:199], v[108:111], v[168:171], v[196:199]\n"
    "v_lshlrev_b32 v206, 16, v177\n"
    "v_and_b32 v205, s46, v176\n"
    "v_mfma_f32_16x16x32_bf16 v[196:199], v[132:135], v[172:175], v[196:199]\n"
    "v_lshlrev_b32 v204, 16, v176\n"
    "v_and_b32 v211, s46, v179\n"
    "v_mfma_f32_16x16x32_bf16 v[200:203], v[88:91], v[160:163], v[200:203]\n"
    "v_lshlrev_b32 v210, 16, v179\n"
    "v_and_b32 v209, s46, v178\n"
    "v_mfma_f32_16x16x32_bf16 v[200:203], v[112:115], v[168:171], v[200:203]\n"
    "v_lshlrev_b32 v208, 16, v178\n"
    "v_cvt_pk_bf16_f32 v213, v190, v191\n"
    "v_mfma_f32_16x16x32_bf16 v[200:203], v[136:139], v[172:175], v[200:203]\n"
    "v_cvt_pk_bf16_f32 v212, v188, v189\n"
    "v_cvt_pk_bf16_f32 v215, v194, v195\n"
    "v_mfma_f32_16x16x32_bf16 v[204:207], v[92:95], v[160:163], v[204:207]\n"
    "v_cvt_pk_bf16_f32 v214, v192, v193\n"
    "v_cvt_pk_bf16_f32 v217, v198, v199\n"
    "v_mfma_f32_16x16x32_bf16 v[204:207], v[116:119], v[168:171], v[204:207]\n"
    "v_cvt_pk_bf16_f32 v216, v196, v197\n"
    "v_mfma_f32_16x16x32_bf16 v[204:207], v[140:143], v[172:175], v[204:207]\n"
    "v_cvt_pk_bf16_f32 v219, v202, v203\n"
    "v_cvt_pk_bf16_f32 v218, v200, v201\n"
    "v_mfma_f32_16x16x32_bf16 v[208:211], v[96:99], v[160:163], v[208:211]\n"
    "v_mfma_f32_16x16x32_bf16 v[208:211], v[120:123], v[168:171], v[208:211]\n"
    "v_mfma_f32_16x16x32_bf16 v[208:211], v[144:147], v[172:175], v[208:211]\n"
    "s_nop 2\n"
    "v_cvt_pk_bf16_f32 v221, v206, v207\n"
    "v_cvt_pk_bf16_f32 v220, v204, v205\n"
    "s_nop 2\n"
    "v_cvt_pk_bf16_f32 v223, v210, v211\n"
    "v_cvt_pk_bf16_f32 v222, v208, v209\n"
    "ds_read_b32 v1, v12 offset:20\n"
    "s_waitcnt lgkmcnt(0)\n"
    "v_cmp_ne_u32 vcc, 0, v1\n"
    "s_nop 0\n"
    "s_cbranch_vccz LG_14\n"
    "LP_15:\n"
    "ds_read_b32 v1, v12 offset:20\n"
    "s_waitcnt lgkmcnt(0)\n"
    "v_cmp_ne_u32 vcc, 0, v1\n"
    "s_sub_u32 s39, s39, 1\n"
    "s_cbranch_scc1 LDEAD\n"
    "s_cbranch_vccnz LP_15\n"
    "LG_14:\n"
    "ds_read_b128 v[224:227], v181 offset:0\n"
    "ds_read_b128 v[228:231], v180 offset:0\n"
    "ds_read_b128 v[232:235], v180 offset:64\n"
    "ds_read_b128 v[236:239], v180 offset:128\n"
    "ds_read_b128 v[240:243], v181 offset:64\n"
    "ds_read_b128 v[244:247], v181 offset:128\n"
    "s_waitcnt lgkmcnt(5)\n"
    "v_and_b32 v251, s46, v225\n"
    "v_lshlrev_b32 v250, 16, v225\n"
    "v_and_b32 v249, s46, v224\n"
    "v_lshlrev_b32 v248, 16, v224\n"
    "v_and_b32 v255, s46, v227\n"
    "s_waitcnt lgkmcnt(4)\n"
    "v_mfma_f32_16x16x32_bf16 v[248:251], v[68:71], v[228:231], v[248:251]\n"
    "v_lshlrev_b32 v254, 16, v227\n"
    "s_waitcnt lgkmcnt(3)\n"
    "v_mfma_f32_16x16x32_bf16 v[248:251], v[100:103], v[232:235], v[248:251]\n"
    "v_and_b32 v253, s46, v226\n"
    "s_waitcnt lgkmcnt(2)\n"
    "v_mfma_f32_16x16x32_bf16 v[248:251], v[124:127], v[236:239], v[248:251]\n"
    "v_lshlrev_b32 v252, 16, v226\n"
    "s_waitcnt lgkmcnt(1)\n"
    "v_and_b32 v23, s46, v241\n"
    "v_mfma_f32_16x16x32_bf16 v[252:255], v[76:79], v[228:231], v[252:255]\n"
    "v_lshlrev_b32 v22, 16, v241\n"
    "v_and_b32 v21, s46, v240\n"
    "v_mfma_f32_16x16x32_bf16 v[252:255], v[104:107], v[232:235], v[252:255]\n"
    "v_lshlrev_b32 v20, 16, v240\n"
    "v_and_b32 v31, s46, v243\n"
    "v_mfma_f32_16x16x32_bf16 v[252:255], v[128:131], v[236:239], v[252:255]\n"
    "v_lshlrev_b32 v30, 16, v243\n"
    "v_and_b32 v29, s46, v242\n"
    "v_mfma_f32_16x16x32_bf16 v[20:23], v[80:83], v[228:231], v[20:23]\n"
    "ds_read_b64_tr_b16 v[38:39], v17 offset:768\n"
    "v_lshlrev_b32 v28, 16, v242\n"
    "v_mfma_f32_16x16x32_bf16 v[20:23], v[108:111], v[232:235], v[20:23]\n"
    "ds_read_b64_tr_b16 v[36:37], v17 offset:0\n"
    "v_cvt_pk_bf16_f32 v41, v250, v251\n"
    "v_mfma_f32_16x16x32_bf16 v[20:23], v[132:135], v[236:239], v[20:23]\n"
    "s_waitcnt lgkmcnt(2)\n"
    "v_and_b32 v35, s46, v245\n"
    "v_mfma_f32_16x16x32_bf16 v[28:31], v[88:91], v[228:231], v[28:31]\n"
    "ds_read_b64_tr_b16 v[46:47], v18 offset:768\n"
    "v_lshlrev_b32 v34, 16, v245\n"
    "v_mfma_f32_16x16x32_bf16 v[28:31], v[112:115], v[232:235], v[28:31]\n"
    "ds_read_b64_tr_b16 v[44:45], v18 offset:0\n"
    "v_and_b32 v33, s46, v244\n"
    "v_mfma_f32_16x16x32_bf16 v[28:31], v[136:139], v[236:239], v[28:31]\n"
    "v_lshlrev_b32 v32, 16, v244\n"
    "v_and_b32 v55, s46, v247\n"
    "v_lshlrev_b32 v54, 16, v247\n"
    "v_mfma_f32_16x16x32_bf16 v[32:35], v[92:95], v[228:231], v[32:35]\n"
    "ds_read_b32 v15, v12 offset:24\n"
    "v_and_b32 v53, s46, v246\n"
    "v_mfma_f32_16x16x32_bf16 v[32:35], v[116:119], v[232:235], v[32:35]\n"
    "ds_read_b64_tr_b16 v[58:59], v17 offset:832\n"
    "v_lshlrev_b32 v52, 16, v246\n"
    "v_mfma_f32_16x16x32_bf16 v[32:35], v[140:143], v[236:239], v[32:35]\n"
    "ds_read_b64_tr_b16 v[56:57], v17 offset:64\n"
    "v_cvt_pk_bf16_f32 v43, v254, v255\n"
    "v_mfma_f32_16x16x32_bf16 v[52:55], v[96:99], v[228:231], v[52:55]\n"
    "ds_read_b64_tr_b16 v[50:51], v18 offset:832\n"
    "v_cvt_pk_bf16_f32 v42, v252, v253\n"
    "v_mfma_f32_16x16x32_bf16 v[52:55], v[120:123], v[232:235], v[52:55]\n"
    "ds_read_b64_tr_b16 v[48:49], v18 offset:64\n"
    "v_cvt_pk_bf16_f32 v40, v248, v249\n"
    "v_mfma_f32_16x16x32_bf16 v[52:55], v[144:147], v[236:239], v[52:55]\n"
    "ds_read_b64_tr_b16 v[26:27], v17 offset:896\n"
    "s_waitcnt lgkmcnt(8)\n"
    "v_mfma_f32_16x16x32_bf16 v[60:63], v[36:39], v[40:43], 0\n"
    "ds_read_b64_tr_b16 v[24:25], v17 offset:128\n"
    "v_cvt_pk_bf16_f32 v65, v22, v23\n"
    "v_mfma_f32_16x16x32_bf16 v[72:75], v[36:39], v[148:151], 0\n"
    "ds_read_b64_tr_b16 v[86:87], v18 offset:896\n"
    "v_cvt_pk_bf16_f32 v64, v20, v21\n"
    "v_mfma_f32_16x16x32_bf16 v[156:159], v[36:39], v[212:215], 0\n"
    "ds_read_b64_tr_b16 v[84:85], v18 offset:128\n"
    "s_waitcnt lgkmcnt(9)\n"
    "v_mfma_f32_16x16x32_bf16 v[164:167], v[44:47], v[40:43], 0\n"
    "s_waitcnt lgkmcnt(8)\n"
    "v_cmp_ne_u32 vcc, 0, v15\n"
    "v_mfma_f32_16x16x32_bf16 v[176:179], v[44:47], v[148:151], 0\n"
    "v_cvt_pk_bf16_f32 v67, v30, v31\n"
    "s_waitcnt lgkmcnt(6)\n"
    "v_mfma_f32_16x16x32_bf16 v[188:191], v[56:59], v[40:43], 0\n"
    "s_cbranch_vccnz LF_16\n"
    "LF_16b:\n"
    "v_mfma_f32_16x16x32_bf16 v[192:195], v[56:59], v[148:151], 0\n"
    "ds_read_b64_tr_b16 v[198:199], v17 offset:6912\n"
    "v_cvt_pk_bf16_f32 v66, v28, v29\n"
    "v_mfma_f32_16x16x32_bf16 v[200:203], v[56:59], v[212:215], 0\n"
    "ds_read_b64_tr_b16 v[196:197], v17 offset:6144\n"
    "s_waitcnt lgkmcnt(6)\n"
    "v_mfma_f32_16x16x32_bf16 v[160:163], v[48:51], v[40:43], 0\n"
    "ds_read_b64_tr_b16 v[170:171], v18 offset:6912\n"
    "v_cvt_pk_bf16_f32 v173, v34, v35\n"
    "v_mfma_f32_16x16x32_bf16 v[204:207], v[48:51], v[148:151], 0\n"
    "ds_read_b64_tr_b16 v[168:169], v18 offset:6144\n"
    "s_waitcnt lgkmcnt(6)\n"
    "v_mfma_f32_16x16x32_bf16 v[208:211], v[24:27], v[40:43], 0\n"
    "ds_read_b32 v1, v12 offset:28\n"
    "v_cvt_pk_bf16_f32 v172, v32, v33\n"
    "v_mfma_f32_16x16x32_bf16 v[180:183], v[24:27], v[148:151], 0\n"
    "ds_read_b64_tr_b16 v[226:227], v17 offset:6976\n"
    "v_cvt_pk_bf16_f32 v175, v54, v55\n"
    "v_mfma_f32_16x16x32_bf16 v[240:243], v[24:27], v[212:215], 0\n"
    "ds_read_b64_tr_b16 v[224:225], v17 offset:6208\n"
    "s_waitcnt lgkmcnt(7)\n"
    "v_mfma_f32_16x16x32_bf16 v[244:247], v[84:87], v[40:43], 0\n"
    "ds_read_b64_tr_b16 v[230:231], v18 offset:6976\n"
    "v_cvt_pk_bf16_f32 v174, v52, v53\n"
    "v_mfma_f32_16x16x32_bf16 v[252:255], v[84:87], v[148:151], 0\n"
    "ds_read_b64_tr_b16 v[228:229], v18 offset:6208\n"
    "ds_read_b64_tr_b16 v[234:235], v17 offset:7040\n"
    "s_waitcnt lgkmcnt(8)\n"
    "v_mfma_f32_16x16x32_bf16 v[60:63], v[196:199], v[64:67], v[60:63]\n"
    "ds_read_b64_tr_b16 v[232:233], v17 offset:6272\n"
    "v_mfma_f32_16x16x32_bf16 v[72:75], v[196:199], v[152:155], v[72:75]\n"
    "ds_read_b64_tr_b16 v[250:251], v18 offset:7040\n"
    "v_mfma_f32_16x16x32_bf16 v[156:159], v[196:199], v[216:219], v[156:159]\n"
    "ds_read_b64_tr_b16 v[248:249], v18 offset:6272\n"
    "s_waitcnt lgkmcnt(9)\n"
    "v_mfma_f32_16x16x32_bf16 v[164:167], v[168:171], v[64:67], v[164:167]\n"
    "s_waitcnt lgkmcnt(8)\n"
    "v_cmp_ne_u32 vcc, 0, v1\n"
    "v_mfma_f32_16x16x32_bf16 v[176:179], v[168:171], v[152:155], v[176:179]\n"
    "ds_read_b32 v15, v12 offset:32\n"
    "s_waitcnt lgkmcnt(7)\n"
    "v_mfma_f32_16x16x32_bf16 v[188:191], v[224:227], v[64:67], v[188:191]\n"
    "s_cbranch_vccnz LF_17\n"
    "LF_17b:\n"
    "v_mfma_f32_16x16x32_bf16 v[192:195], v[224:227], v[152:155], v[192:195]\n"
    "ds_read_b64_tr_b16 v[238:239], v17 offset:13056\n"
    "v_add_u32 v21, s41, v11\n"
    "v_mfma_f32_16x16x32_bf16 v[200:203], v[224:227], v[216:219], v[200:203]\n"
    "ds_read_b64_tr_b16 v[236:237], v17 offset:12288\n"
    "s_waitcnt lgkmcnt(7)\n"
    "v_mfma_f32_16x16x32_bf16 v[160:163], v[228:231], v[64:67], v[160:163]\n"
    "ds_read_b64_tr_b16 v[38:39], v18 offset:13056\n"
    "v_mad_i32_i24 v20, s11, 1, v10\n"
    "v_mfma_f32_16x16x32_bf16 v[204:207], v[228:231], v[152:155], v[204:207]\n"
    "ds_read_b64_tr_b16 v[36:37], v18 offset:12288\n"
    "s_waitcnt lgkmcnt(7)\n"
    "v_mfma_f32_16x16x32_bf16 v[208:211], v[232:235], v[64:67], v[208:211]\n"
    "ds_read_b64_tr_b16 v[46:47], v17 offset:13120\n"
    "v_mfma_f32_16x16x32_bf16 v[180:183], v[232:235], v[152:155], v[180:183]\n"
    "ds_read_b64_tr_b16 v[44:45], v17 offset:12352\n"
    "v_mfma_f32_16x16x32_bf16 v[240:243], v[232:235], v[216:219], v[240:243]\n"
    "ds_read_b64_tr_b16 v[30:31], v18 offset:13120\n"
    "s_waitcnt lgkmcnt(8)\n"
    "v_mfma_f32_16x16x32_bf16 v[244:247], v[248:251], v[64:67], v[244:247]\n"
    "LPT0:\n"
    "s_cbranch_execz LCS\n"
    "ds_read_b64_tr_b16 v[28:29], v18 offset:12352\n"
    "v_mfma_f32_16x16x32_bf16 v[252:255], v[248:251], v[152:155], v[252:255]\n"
    "ds_read_b64_tr_b16 v[58:59], v17 offset:13184\n"
    "s_waitcnt lgkmcnt(9)\n"
    "v_cmp_ne_u32 vcc, 0, v15\n"
    "s_waitcnt lgkmcnt(7)\n"
    "v_mfma_f32_16x16x32_bf16 v[60:63], v[236:239], v[172:175], v[60:63]\n"
    "ds_read_b64_tr_b16 v[56:57], v17 offset:12416\n"
    "v_mfma_f32_16x16x32_bf16 v[72:75], v[236:239], v[184:187], v[72:75]\n"
    "ds_read_b64_tr_b16 v[50:51], v18 offset:13184\n"
    "v_mfma_f32_16x16x32_bf16 v[156:159], v[236:239], v[220:223], v[156:159]\n"
    "ds_read_b64_tr_b16 v[48:49], v18 offset:12416\n"
    "s_waitcnt lgkmcnt(8)\n"
    "v_mfma_f32_16x16x32_bf16 v[164:167], v[36:39], v[172:175], v[164:167]\n"
    "s_cbranch_vccnz LF_18\n"
    "LF_18b:\n"
    "v_mfma_f32_16x16x32_bf16 v[176:179], v[36:39], v[184:187], v[176:179]\n"
    "ds_read_b128 v[32:35], v21 offset:0\n"
    "s_waitcnt lgkmcnt(7)\n"
    "v_mfma_f32_16x16x32_bf16 v[188:191], v[44:47], v[172:175], v[188:191]\n"
    "ds_read_b128 v[24:27], v20 offset:0\n"
    "v_mfma_f32_16x16x32_bf16 v[192:195], v[44:47], v[184:187], v[192:195]\n"
    "ds_read_b128 v[52:55], v20 offset:64\n"
    "v_mfma_f32_16x16x32_bf16 v[200:203], v[44:47], v[220:223], v[200:203]\n"
    "ds_read_b128 v[84:87], v20 offset:128\n"
    "s_waitcnt lgkmcnt(8)\n"
    "v_mfma_f32_16x16x32_bf16 v[160:163], v[28:31], v[172:175], v[160:163]\n"
    "ds_read_b128 v[196:199], v21 offset:64\n"
    "v_mfma_f32_16x16x32_bf16 v[204:207], v[28:31], v[184:187], v[204:207]\n"
    "ds_read_b32 v1, v12 offset:36\n"
    "s_waitcnt lgkmcnt(8)\n"
    "v_mfma_f32_16x16x32_bf16 v[208:211], v[56:59], v[172:175], v[208:211]\n"
    "ds_read_b128 v[168:171], v21 offset:128\n"
    "v_mfma_f32_16x16x32_bf16 v[180:183], v[56:59], v[184:187], v[180:183]\n"
    "v_mfma_f32_16x16x32_bf16 v[240:243], v[56:59], v[220:223], v[240:243]\n"
    "v_add_u32 v225, s40, v11\n"
    "s_waitcnt lgkmcnt(7)\n"
    "v_mfma_f32_16x16x32_bf16 v[244:247], v[48:51], v[172:175], v[244:247]\n"
    "s_waitcnt lgkmcnt(6)\n"
    "v_and_b32 v231, s46, v33\n"
    "v_mfma_f32_16x16x32_bf16 v[252:255], v[48:51], v[184:187], v[252:255]\n"
    "v_lshlrev_b32 v230, 16, v33\n"
    "v_and_b32 v229, s46, v32\n"
    "v_lshlrev_b32 v228, 16, v32\n"
    "v_and_b32 v235, s46, v35\n"
    "s_waitcnt lgkmcnt(5)\n"
    "v_mfma_f32_16x16x32_bf16 v[228:231], v[68:71], v[24:27], v[228:231]\n"
    "v_lshlrev_b32 v234, 16, v35\n"
    "s_waitcnt lgkmcnt(4)\n"
    "v_mfma_f32_16x16x32_bf16 v[228:231], v[100:103], v[52:55], v[228:231]\n"
    "v_and_b32 v233, s46, v34\n"
    "s_waitcnt lgkmcnt(3)\n"
    "v_mfma_f32_16x16x32_bf16 v[228:231], v[124:127], v[84:87], v[228:231]\n"
    "v_lshlrev_b32 v232, 16, v34\n"
    "s_waitcnt lgkmcnt(2)\n"
    "v_and_b32 v251, s46, v197\n"
    "v_mfma_f32_16x16x32_bf16 v[232:235], v[76:79], v[24:27], v[232:235]\n"
    "v_lshlrev_b32 v250, 16, v197\n"
    "v_and_b32 v249, s46, v196\n"
    "v_mfma_f32_16x16x32_bf16 v[232:235], v[104:107], v[52:55], v[232:235]\n"
    "s_waitcnt lgkmcnt(0)\n"
    "v_cmp_ne_u32 vcc, 0, v1\n"
    "v_mfma_f32_16x16x32_bf16 v[232:235], v[128:131], v[84:87], v[232:235]\n"
    "v_lshlrev_b32 v248, 16, v196\n"
    "v_and_b32 v239, s46, v199\n"
    "v_lshlrev_b32 v238, 16, v199\n"
    "v_mfma_f32_16x16x32_bf16 v[248:251], v[80:83], v[24:27], v[248:251]\n"
    "s_cbranch_vccnz LF_19\n"
    "LF_19b:\n"
    "v_mfma_f32_16x16x32_bf16 v[248:251], v[108:111], v[52:55], v[248:251]\n"
    "ds_read_b128 v[36:39], v225 offset:0\n"
    "v_and_b32 v237, s46, v198\n"
    "v_mfma_f32_16x16x32_bf16 v[248:251], v[132:135], v[84:87], v[248:251]\n"
    "v_lshlrev_b32 v236, 16, v198\n"
    "v_mad_i32_i24 v224, s11, 0, v10\n"
    "v_and_b32 v47, s46, v169\n"
    "v_mfma_f32_16x16x32_bf16 v[236:239], v[88:91], v[24:27], v[236:239]\n"
    "ds_read_b128 v[28:31], v224 offset:0\n"
    "v_lshlrev_b32 v46, 16, v169\n"
    "v_mfma_f32_16x16x32_bf16 v[236:239], v[112:115], v[52:55], v[236:239]\n"
    "ds_read_b128 v[20:23], v224 offset:64\n"
    "v_and_b32 v45, s46, v168\n"
    "v_mfma_f32_16x16x32_bf16 v[236:239], v[136:139], v[84:87], v[236:239]\n"
    "v_lshlrev_b32 v44, 16, v168\n"
    "ds_read_b128 v[56:59], v224 offset:128\n"
    "s_nop 0\n"
    "v_mfma_f32_16x16x32_bf16 v[44:47], v[92:95], v[24:27], v[44:47]\n"
    "ds_read_b128 v[48:51], v225 offset:64\n"
    "v_and_b32 v35, s46, v171\n"
    "v_mfma_f32_16x16x32_bf16 v[44:47], v[116:119], v[52:55], v[44:47]\n"
    "v_lshlrev_b32 v34, 16, v171\n"
    "v_and_b32 v33, s46, v170\n"
    "v_mfma_f32_16x16x32_bf16 v[44:47], v[140:143], v[84:87], v[44:47]\n"
    "v_lshlrev_b32 v32, 16, v170\n"
    "v_cvt_pk_bf16_f32 v197, v230, v231\n"
    "v_cvt_pk_bf16_f32 v196, v228, v229\n"
    "v_mfma_f32_16x16x32_bf16 v[32:35], v[96:99], v[24:27], v[32:35]\n"
    "s_waitcnt lgkmcnt(4)\n"
    "v_and_b32 v171, s46, v37\n"
    "v_mfma_f32_16x16x32_bf16 v[32:35], v[120:123], v[52:55], v[32:35]\n"
    "v_lshlrev_b32 v170, 16, v37\n"
    "v_and_b32 v169, s46, v36\n"
    "v_mfma_f32_16x16x32_bf16 v[32:35], v[144:147], v[84:87], v[32:35]\n"
    "v_lshlrev_b32 v168, 16, v36\n"
    "ds_read_b128 v[228:231], v225 offset:128\n"
    "s_waitcnt lgkmcnt(4)\n"
    "v_mfma_f32_16x16x32_bf16 v[168:171], v[68:71], v[28:31], v[168:171]\n"
    "ds_read_b32 v15, v12 offset:40\n"
    "s_waitcnt lgkmcnt(4)\n"
    "v_mfma_f32_16x16x32_bf16 v[168:171], v[100:103], v[20:23], v[168:171]\n"
    "v_and_b32 v27, s46, v39\n"
    "s_waitcnt lgkmcnt(3)\n"
    "v_mfma_f32_16x16x32_bf16 v[168:171], v[124:127], v[56:59], v[168:171]\n"
    "ds_read_b64_tr_b16 v[54:55], v17 offset:768\n"
    "v_lshlrev_b32 v26, 16, v39\n"
    "v_and_b32 v25, s46, v38\n"
    "v_lshlrev_b32 v24, 16, v38\n"
    "ds_read_b64_tr_b16 v[52:53], v17 offset:0\n"
    "s_nop 0\n"
    "v_mfma_f32_16x16x32_bf16 v[24:27], v[76:79], v[28:31], v[24:27]\n"
    "ds_read_b64_tr_b16 v[86:87], v17 offset:6912\n"
    "v_cvt_pk_bf16_f32 v199, v234, v235\n"
    "v_mfma_f32_16x16x32_bf16 v[24:27], v[104:107], v[20:23], v[24:27]\n"
    "ds_read_b64_tr_b16 v[84:85], v17 offset:6144\n"
    "v_cvt_pk_bf16_f32 v198, v232, v233\n"
    "v_mfma_f32_16x16x32_bf16 v[24:27], v[128:131], v[56:59], v[24:27]\n"
    "s_waitcnt lgkmcnt(6)\n"
    "v_and_b32 v227, s46, v49\n"
    "v_lshlrev_b32 v226, 16, v49\n"
    "v_and_b32 v225, s46, v48\n"
    "v_lshlrev_b32 v224, 16, v48\n"
    "ds_read_b64_tr_b16 v[70:71], v17 offset:13056\n"
    "s_nop 0\n"
    "v_mfma_f32_16x16x32_bf16 v[224:227], v[80:83], v[28:31], v[224:227]\n"
    "ds_read_b64_tr_b16 v[68:69], v17 offset:12288\n"
    "v_cvt_pk_bf16_f32 v101, v250, v251\n"
    "v_mfma_f32_16x16x32_bf16 v[224:227], v[108:111], v[20:23], v[224:227]\n"
    "v_cvt_pk_bf16_f32 v100, v248, v249\n"
    "v_and_b32 v127, s46, v51\n"
    "v_mfma_f32_16x16x32_bf16 v[224:227], v[132:135], v[56:59], v[224:227]\n"
    "s_waitcnt lgkmcnt(6)\n"
    "v_cmp_ne_u32 vcc, 0, v15\n"
    "v_lshlrev_b32 v126, 16, v51\n"
    "v_and_b32 v125, s46, v50\n"
    "v_lshlrev_b32 v124, 16, v50\n"
    "v_cvt_pk_bf16_f32 v103, v238, v239\n"
    "v_cvt_pk_bf16_f32 v102, v236, v237\n"
    "v_mfma_f32_16x16x32_bf16 v[124:127], v[88:91], v[28:31], v[124:127]\n"
    "v_and_b32 v39, s46, v229\n"
    "v_lshlrev_b32 v38, 16, v229\n"
    "v_mfma_f32_16x16x32_bf16 v[124:127], v[112:115], v[20:23], v[124:127]\n"
    "s_cbranch_vccnz LF_20\n"
    "LF_20b:\n"
    "v_mfma_f32_16x16x32_bf16 v[124:127], v[136:139], v[56:59], v[124:127]\n"
    "ds_read_b64_tr_b16 v[78:79], v8 offset:768\n"
    "ds_read_b64_tr_b16 v[76:77], v8 offset:0\n"
    "ds_read_b64_tr_b16 v[106:107], v2 offset:768\n"
    "ds_read_b64_tr_b16 v[104:105], v2 offset:0\n"
    "v_and_b32 v37, s46, v228\n"
    "v_lshlrev_b32 v36, 16, v228\n"
    "ds_read_b64_tr_b16 v[234:235], v18 offset:768\n"
    "s_nop 0\n"
    "v_mfma_f32_16x16x32_bf16 v[36:39], v[92:95], v[28:31], v[36:39]\n"
    "ds_read_b64_tr_b16 v[232:233], v18 offset:0\n"
    "v_cvt_pk_bf16_f32 v129, v46, v47\n"
    "v_mfma_f32_16x16x32_bf16 v[36:39], v[116:119], v[20:23], v[36:39]\n"
    "v_cvt_pk_bf16_f32 v128, v44, v45\n"
    "v_and_b32 v83, s46, v231\n"
    "v_mfma_f32_16x16x32_bf16 v[36:39], v[140:143], v[56:59], v[36:39]\n"
    "v_lshlrev_b32 v82, 16, v231\n"
    "v_and_b32 v81, s46, v230\n"
    "v_lshlrev_b32 v80, 16, v230\n"
    "ds_read_b64_tr_b16 v[110:111], v18 offset:6912\n"
    "s_nop 0\n"
    "v_mfma_f32_16x16x32_bf16 v[80:83], v[96:99], v[28:31], v[80:83]\n"
    "ds_read_b64_tr_b16 v[108:109], v18 offset:6144\n"
    "v_cvt_pk_bf16_f32 v131, v34, v35\n"
    "v_mfma_f32_16x16x32_bf16 v[80:83], v[120:123], v[20:23], v[80:83]\n"
    "ds_read_b64_tr_b16 v[250:251], v18 offset:13056\n"
    "v_cvt_pk_bf16_f32 v130, v32, v33\n"
    "v_mfma_f32_16x16x32_bf16 v[80:83], v[144:147], v[56:59], v[80:83]\n"
    "s_barrier\n"
    "s_waitcnt lgkmcnt(7)\n"
    "v_mfma_f32_16x16x32_bf16 v[132:135], v[52:55], v[196:199], 0\n"
    "ds_read_b64_tr_b16 v[248:249], v18 offset:12288\n"
    "v_cvt_pk_bf16_f32 v49, v170, v171\n"
    "v_mfma_f32_16x16x32_bf16 v[132:135], v[84:87], v[100:103], v[132:135]\n"
    "ds_read_b64_tr_b16 v[238:239], v9 offset:768\n"
    "v_cvt_pk_bf16_f32 v48, v168, v169\n"
    "v_mfma_f32_16x16x32_bf16 v[132:135], v[68:71], v[128:131], v[132:135]\n"
    "ds_read_b64_tr_b16 v[236:237], v9 offset:0\n"
    "ds_read_b64_tr_b16 v[90:91], v3 offset:768\n"
    "v_cvt_pk_bf16_f32 v51, v26, v27\n"
    "v_cvt_pk_bf16_f32 v50, v24, v25\n"
    "ds_read_b64_tr_b16 v[88:89], v3 offset:0\n"
    "s_nop 0\n"
    "v_mfma_f32_16x16x32_bf16 v[132:135], v[76:79], v[48:51], v[132:135]\n"
    "ds_read_b64_tr_b16 v[114:115], v17 offset:832\n"
    "s_waitcnt lgkmcnt(9)\n"
    "v_mfma_f32_16x16x32_bf16 v[132:135], v[104:107], v[40:43], v[132:135]\n"
    "ds_read_b64_tr_b16 v[112:113], v17 offset:64\n"
    "v_cvt_pk_bf16_f32 v137, v226, v227\n"
    "v_mfma_f32_16x16x32_bf16 v[60:63], v[76:79], v[196:199], v[60:63]\n"
    "ds_read_b64_tr_b16 v[94:95], v17 offset:6976\n"
    "v_cvt_pk_bf16_f32 v136, v224, v225\n"
    "v_mfma_f32_16x16x32_bf16 v[60:63], v[104:107], v[148:151], v[60:63]\n"
    "ds_read_b64_tr_b16 v[92:93], v17 offset:6208\n"
    "v_cvt_pk_bf16_f32 v139, v126, v127\n"
    "v_mfma_f32_16x16x32_bf16 v[72:75], v[76:79], v[40:43], v[72:75]\n"
    "ds_read_b64_tr_b16 v[118:119], v17 offset:13120\n"
    "v_cvt_pk_bf16_f32 v138, v124, v125\n"
    "v_mfma_f32_16x16x32_bf16 v[72:75], v[104:107], v[212:215], v[72:75]\n"
    "ds_read_b64_tr_b16 v[116:117], v17 offset:12352\n"
    "v_cvt_pk_bf16_f32 v45, v38, v39\n"
    "v_mfma_f32_16x16x32_bf16 v[156:159], v[76:79], v[148:151], v[156:159]\n"
    "ds_read_b64_tr_b16 v[142:143], v8 offset:832\n"
    "v_cvt_pk_bf16_f32 v44, v36, v37\n"
    "v_mfma_f32_16x16x32_bf16 v[228:231], v[232:235], v[196:199], 0\n"
    "ds_read_b64_tr_b16 v[140:141], v8 offset:64\n"
    "s_waitcnt lgkmcnt(10)\n"
    "v_mfma_f32_16x16x32_bf16 v[228:231], v[108:111], v[100:103], v[228:231]\n"
    "ds_read_b64_tr_b16 v[98:99], v2 offset:832\n"
    "v_cvt_pk_bf16_f32 v47, v82, v83\n"
    "v_mfma_f32_16x16x32_bf16 v[228:231], v[248:251], v[128:131], v[228:231]\n"
    "ds_read_b64_tr_b16 v[96:97], v2 offset:64\n"
    "v_cvt_pk_bf16_f32 v46, v80, v81\n"
    "v_mfma_f32_16x16x32_bf16 v[228:231], v[236:239], v[48:51], v[228:231]\n"
    "ds_read_b64_tr_b16 v[30:31], v18 offset:832\n"
    "s_waitcnt lgkmcnt(10)\n"
    "v_mfma_f32_16x16x32_bf16 v[228:231], v[88:91], v[40:43], v[228:231]\n"
    "ds_read_b64_tr_b16 v[28:29], v18 offset:64\n"
    "v_mfma_f32_16x16x32_bf16 v[164:167], v[236:239], v[196:199], v[164:167]\n"
    "ds_read_b64_tr_b16 v[122:123], v18 offset:6976\n"
    "v_mfma_f32_16x16x32_bf16 v[164:167], v[88:91], v[148:151], v[164:167]\n"
    "ds_read_b64_tr_b16 v[120:121], v18 offset:6208\n"
    "v_mfma_f32_16x16x32_bf16 v[176:179], v[236:239], v[40:43], v[176:179]\n"
    "ds_read_b64_tr_b16 v[22:23], v18 offset:13120\n"
    "v_mfma_f32_16x16x32_bf16 v[176:179], v[88:91], v[212:215], v[176:179]\n"
    "ds_read_b64_tr_b16 v[20:21], v18 offset:12352\n"
    "v_mfma_f32_16x16x32_bf16 v[32:35], v[236:239], v[148:151], 0\n"
    "ds_read_b64_tr_b16 v[146:147], v9 offset:832\n"
    "s_waitcnt lgkmcnt(10)\n"
    "v_mfma_f32_16x16x32_bf16 v[56:59], v[112:115], v[196:199], 0\n"
    "ds_read_b64_tr_b16 v[144:145], v9 offset:64\n"
    "v_mfma_f32_16x16x32_bf16 v[56:59], v[92:95], v[100:103], v[56:59]\n"
    "ds_read_b64_tr_b16 v[54:55], v3 offset:832\n"
    "v_mfma_f32_16x16x32_bf16 v[56:59], v[116:119], v[128:131], v[56:59]\n"
    "ds_read_b64_tr_b16 v[52:53], v3 offset:64\n"
    "s_waitcnt lgkmcnt(10)\n"
    "v_mfma_f32_16x16x32_bf16 v[56:59], v[140:143], v[48:51], v[56:59]\n"
    "ds_read_b64_tr_b16 v[86:87], v17 offset:896\n"
    "v_mfma_f32_16x16x32_bf16 v[56:59], v[96:99], v[40:43], v[56:59]\n"
    "ds_read_b64_tr_b16 v[84:85], v17 offset:128\n"
    "v_mfma_f32_16x16x32_bf16 v[188:191], v[140:143], v[196:199], v[188:191]\n"
    "ds_read_b64_tr_b16 v[170:171], v17 offset:7040\n"
    "v_mfma_f32_16x16x32_bf16 v[188:191], v[96:99], v[148:151], v[188:191]\n"
    "ds_read_b64_tr_b16 v[168:169], v17 offset:6272\n"
    "v_mfma_f32_16x16x32_bf16 v[192:195], v[140:143], v[40:43], v[192:195]\n"
    "ds_read_b64_tr_b16 v[70:71], v17 offset:13184\n"
    "v_mfma_f32_16x16x32_bf16 v[192:195], v[96:99], v[212:215], v[192:195]\n"
    "ds_read_b64_tr_b16 v[68:69], v17 offset:12416\n"
    "v_mfma_f32_16x16x32_bf16 v[200:203], v[140:143], v[148:151], v[200:203]\n"
    "ds_read_b64_tr_b16 v[26:27], v8 offset:896\n"
    "s_waitcnt lgkmcnt(10)\n"
    "v_mfma_f32_16x16x32_bf16 v[224:227], v[28:31], v[196:199], 0\n"
    "ds_read_b64_tr_b16 v[24:25], v8 offset:128\n"
    "v_mfma_f32_16x16x32_bf16 v[224:227], v[120:123], v[100:103], v[224:227]\n"
    "ds_read_b64_tr_b16 v[126:127], v2 offset:896\n"
    "v_mfma_f32_16x16x32_bf16 v[224:227], v[20:23], v[128:131], v[224:227]\n"
    "ds_read_b64_tr_b16 v[124:125], v2 offset:128\n"
    "s_waitcnt lgkmcnt(10)\n"
    "v_mfma_f32_16x16x32_bf16 v[224:227], v[144:147], v[48:51], v[224:227]\n"
    "ds_read_b32 v1, v12 offset:44\n"
    "v_mfma_f32_16x16x32_bf16 v[224:227], v[52:55], v[40:43], v[224:227]\n"
    "ds_read_b64_tr_b16 v[106:107], v9 offset:896\n"
    "v_mfma_f32_16x16x32_bf16 v[160:163], v[144:147], v[196:199], v[160:163]\n"
    "ds_read_b64_tr_b16 v[104:105], v9 offset:128\n"
    "v_mfma_f32_16x16x32_bf16 v[160:163], v[52:55], v[148:151], v[160:163]\n"
    "ds_read_b32 v15, v12 offset:48\n"
    "v_mfma_f32_16x16x32_bf16 v[204:207], v[144:147], v[40:43], v[204:207]\n"
    "ds_read_b64_tr_b16 v[78:79], v18 offset:896\n"
    "v_mfma_f32_16x16x32_bf16 v[204:207], v[52:55], v[212:215], v[204:207]\n"
    "ds_read_b64_tr_b16 v[76:77], v18 offset:128\n"
    "v_mfma_f32_16x16x32_bf16 v[36:39], v[144:147], v[148:151], 0\n"
    "ds_read_b64_tr_b16 v[234:235], v18 offset:7040\n"
    "s_waitcnt lgkmcnt(10)\n"
    "v_mfma_f32_16x16x32_bf16 v[108:111], v[84:87], v[196:199], 0\n"
    "ds_read_b64_tr_b16 v[232:233], v18 offset:6272\n"
    "v_mfma_f32_16x16x32_bf16 v[108:111], v[168:171], v[100:103], v[108:111]\n"
    "ds_read_b64_tr_b16 v[250:251], v18 offset:13184\n"
    "v_mfma_f32_16x16x32_bf16 v[108:111], v[68:71], v[128:131], v[108:111]\n"
    "ds_read_b64_tr_b16 v[248:249], v18 offset:12416\n"
    "s_waitcnt lgkmcnt(10)\n"
    "v_mfma_f32_16x16x32_bf16 v[108:111], v[24:27], v[48:51], v[108:111]\n"
    "ds_read_b64_tr_b16 v[82:83], v3 offset:896\n"
    "s_waitcnt lgkmcnt(10)\n"
    "v_cmp_ne_u32 vcc, 0, v1\n"
    "v_mfma_f32_16x16x32_bf16 v[108:111], v[124:127], v[40:43], v[108:111]\n"
    "ds_read_b64_tr_b16 v[80:81], v3 offset:128\n"
    "v_mfma_f32_16x16x32_bf16 v[208:211], v[24:27], v[196:199], v[208:211]\n"
    "s_cbranch_vccnz LF_21\n"
    "LF_21b:\n"
    "v_mfma_f32_16x16x32_bf16 v[208:211], v[124:127], v[148:151], v[208:211]\n"
    "ds_read_b64_tr_b16 v[90:91], v8 offset:6912\n"
    "s_waitcnt lgkmcnt(9)\n"
    "v_cmp_ne_u32 vcc, 0, v15\n"
    "v_mfma_f32_16x16x32_bf16 v[180:183], v[24:27], v[40:43], v[180:183]\n"
    "ds_read_b64_tr_b16 v[88:89], v8 offset:6144\n"
    "v_mfma_f32_16x16x32_bf16 v[180:183], v[124:127], v[212:215], v[180:183]\n"
    "s_cbranch_vccnz LF_22\n"
    "LF_22b:\n"
    "v_mfma_f32_16x16x32_bf16 v[240:243], v[24:27], v[148:151], v[240:243]\n"
    "ds_read_b64_tr_b16 v[238:239], v8 offset:13056\n"
    "v_mfma_f32_16x16x32_bf16 v[112:115], v[104:107], v[148:151], 0\n"
    "ds_read_b64_tr_b16 v[236:237], v8 offset:12288\n"
    "s_waitcnt lgkmcnt(7)\n"
    "v_mfma_f32_16x16x32_bf16 v[92:95], v[76:79], v[196:199], 0\n"
    "ds_read_b64_tr_b16 v[118:119], v2 offset:6912\n"
    "v_mfma_f32_16x16x32_bf16 v[92:95], v[232:235], v[100:103], v[92:95]\n"
    "ds_read_b64_tr_b16 v[116:117], v2 offset:6144\n"
    "s_waitcnt lgkmcnt(7)\n"
    "v_mfma_f32_16x16x32_bf16 v[92:95], v[248:251], v[128:131], v[92:95]\n"
    "ds_read_b64_tr_b16 v[98:99], v2 offset:13056\n"
    "v_mfma_f32_16x16x32_bf16 v[92:95], v[104:107], v[48:51], v[92:95]\n"
    "ds_read_b64_tr_b16 v[96:97], v2 offset:12288\n"
    "s_waitcnt lgkmcnt(8)\n"
    "v_mfma_f32_16x16x32_bf16 v[92:95], v[80:83], v[40:43], v[92:95]\n"
    "v_mov_b32_dpp v51, v51 row_shl:1 row_mask:0xf bank_mask:0xf bound_ctrl:0\n"
    "v_mov_b32_dpp v50, v50 row_shl:1 row_mask:0xf bank_mask:0xf bound_ctrl:0\n"
    "v_mfma_f32_16x16x32_bf16 v[244:247], v[104:107], v[196:199], v[244:247]\n"
    "ds_read_b64_tr_b16 v[142:143], v9 offset:6912\n"
    "v_mov_b32_dpp v49, v49 row_shl:1 row_mask:0xf bank_mask:0xf bound_ctrl:0\n"
    "v_mfma_f32_16x16x32_bf16 v[244:247], v[80:83], v[148:151], v[244:247]\n"
    "ds_read_b64_tr_b16 v[140:141], v9 offset:6144\n"
    "v_mov_b32_dpp v48, v48 row_shl:1 row_mask:0xf bank_mask:0xf bound_ctrl:0\n"
    "v_mfma_f32_16x16x32_bf16 v[252:255], v[104:107], v[40:43], v[252:255]\n"
    "ds_read_b64_tr_b16 v[30:31], v9 offset:13056\n"
    "v_mov_b32_dpp v199, v199 row_shl:1 row_mask:0xf bank_mask:0xf bound_ctrl:0\n"
    "v_mfma_f32_16x16x32_bf16 v[252:255], v[80:83], v[212:215], v[252:255]\n"
    "ds_read_b64_tr_b16 v[28:29], v9 offset:12288\n"
    "s_waitcnt lgkmcnt(9)\n"
    "v_mfma_f32_16x16x32_bf16 v[132:135], v[88:91], v[136:139], v[132:135]\n"
    "ds_read_b64_tr_b16 v[122:123], v3 offset:6912\n"
    "s_waitcnt lgkmcnt(9)\n"
    "v_mfma_f32_16x16x32_bf16 v[132:135], v[236:239], v[44:47], v[132:135]\n"
    "ds_read_b64_tr_b16 v[120:121], v3 offset:6144\n"
    "s_waitcnt lgkmcnt(8)\n"
    "v_mfma_f32_16x16x32_bf16 v[132:135], v[116:119], v[64:67], v[132:135]\n"
    "ds_read_b64_tr_b16 v[22:23], v3 offset:13056\n"
    "s_waitcnt lgkmcnt(7)\n"
    "v_mfma_f32_16x16x32_bf16 v[132:135], v[96:99], v[172:175], v[132:135]\n"
    "ds_read_b64_tr_b16 v[20:21], v3 offset:12288\n"
    "v_mov_b32_dpp v198, v198 row_shl:1 row_mask:0xf bank_mask:0xf bound_ctrl:0\n"
    "v_mfma_f32_16x16x32_bf16 v[60:63], v[88:91], v[100:103], v[60:63]\n"
    "v_mov_b32_dpp v197, v197 row_shl:1 row_mask:0xf bank_mask:0xf bound_ctrl:0\n"
    "v_mov_b32_dpp v196, v196 row_shl:1 row_mask:0xf bank_mask:0xf bound_ctrl:0\n"
    "v_mfma_f32_16x16x32_bf16 v[60:63], v[236:239], v[128:131], v[60:63]\n"
    "v_mov_b32_dpp v43, v43 row_shl:1 row_mask:0xf bank_mask:0xf bound_ctrl:0\n"
    "v_mov_b32_dpp v42, v42 row_shl:1 row_mask:0xf bank_mask:0xf bound_ctrl:0\n"
    "v_mfma_f32_16x16x32_bf16 v[60:63], v[116:119], v[152:155], v[60:63]\n"
    "v_mov_b32_dpp v41, v41 row_shl:1 row_mask:0xf bank_mask:0xf bound_ctrl:0\n"
    "v_mov_b32_dpp v40, v40 row_shl:1 row_mask:0xf bank_mask:0xf bound_ctrl:0\n"
    "v_mfma_f32_16x16x32_bf16 v[60:63], v[96:99], v[184:187], v[60:63]\n"
    "v_mov_b32_dpp v151, v151 row_shl:1 row_mask:0xf bank_mask:0xf bound_ctrl:0\n"
    "v_mov_b32_dpp v150, v150 row_shl:1 row_mask:0xf bank_mask:0xf bound_ctrl:0\n"
    "v_mfma_f32_16x16x32_bf16 v[72:75], v[88:91], v[64:67], v[72:75]\n"
    "v_mov_b32_dpp v149, v149 row_shl:1 row_mask:0xf bank_mask:0xf bound_ctrl:0\n"
    "v_mov_b32_dpp v148, v148 row_shl:1 row_mask:0xf bank_mask:0xf bound_ctrl:0\n"
    "v_mfma_f32_16x16x32_bf16 v[72:75], v[236:239], v[172:175], v[72:75]\n"
    "v_mov_b32_dpp v215, v215 row_shl:1 row_mask:0xf bank_mask:0xf bound_ctrl:0\n"
    "v_mov_b32_dpp v214, v214 row_shl:1 row_mask:0xf bank_mask:0xf bound_ctrl:0\n"
    "v_mfma_f32_16x16x32_bf16 v[72:75], v[116:119], v[216:219], v[72:75]\n"
    "ds_read_b64_tr_b16 v[54:55], v8 offset:6976\n"
    "v_mov_b32_dpp v213, v213 row_shl:1 row_mask:0xf bank_mask:0xf bound_ctrl:0\n"
    "v_mfma_f32_16x16x32_bf16 v[72:75], v[96:99], v[220:223], v[72:75]\n"
    "ds_read_b64_tr_b16 v[52:53], v8 offset:6208\n"
    "v_mov_b32_dpp v212, v212 row_shl:1 row_mask:0xf bank_mask:0xf bound_ctrl:0\n"
    "v_mfma_f32_16x16x32_bf16 v[156:159], v[88:91], v[152:155], v[156:159]\n"
    "ds_read_b64_tr_b16 v[146:147], v8 offset:13120\n"
    "v_mfma_f32_16x16x32_bf16 v[156:159], v[236:239], v[184:187], v[156:159]\n"
    "ds_read_b64_tr_b16 v[144:145], v8 offset:12352\n"
    "s_waitcnt lgkmcnt(4)\n"
    "v_mfma_f32_16x16x32_bf16 v[228:231], v[140:143], v[136:139], v[228:231]\n"
    "ds_read_b64_tr_b16 v[86:87], v2 offset:6976\n"
    "v_mfma_f32_16x16x32_bf16 v[228:231], v[28:31], v[44:47], v[228:231]\n"
    "ds_read_b64_tr_b16 v[84:85], v2 offset:6208\n"
    "v_mfma_f32_16x16x32_bf16 v[228:231], v[120:123], v[64:67], v[228:231]\n"
    "ds_read_b64_tr_b16 v[170:171], v2 offset:13120\n"
    "v_mfma_f32_16x16x32_bf16 v[228:231], v[20:23], v[172:175], v[228:231]\n"
    "ds_read_b64_tr_b16 v[168:169], v2 offset:12352\n"
    "v_mfma_f32_16x16x32_bf16 v[164:167], v[140:143], v[100:103], v[164:167]\n"
    "v_mfma_f32_16x16x32_bf16 v[164:167], v[28:31], v[128:131], v[164:167]\n"
    "v_mfma_f32_16x16x32_bf16 v[164:167], v[120:123], v[152:155], v[164:167]\n"
    "v_mfma_f32_16x16x32_bf16 v[164:167], v[20:23], v[184:187], v[164:167]\n"
    "v_mfma_f32_16x16x32_bf16 v[176:179], v[140:143], v[64:67], v[176:179]\n"
    "v_mfma_f32_16x16x32_bf16 v[176:179], v[28:31], v[172:175], v[176:179]\n"
    "v_mfma_f32_16x16x32_bf16 v[176:179], v[120:123], v[216:219], v[176:179]\n"
    "ds_read_b64_tr_b16 v[70:71], v9 offset:6976\n"
    "v_mfma_f32_16x16x32_bf16 v[176:179], v[20:23], v[220:223], v[176:179]\n"
    "ds_read_b64_tr_b16 v[68:69], v9 offset:6208\n"
    "v_mfma_f32_16x16x32_bf16 v[32:35], v[140:143], v[152:155], v[32:35]\n"
    "ds_read_b64_tr_b16 v[126:127], v9 offset:13120\n"
    "v_mfma_f32_16x16x32_bf16 v[32:35], v[28:31], v[184:187], v[32:35]\n"
    "ds_read_b64_tr_b16 v[124:125], v9 offset:12352\n"
    "s_waitcnt lgkmcnt(4)\n"
    "v_mfma_f32_16x16x32_bf16 v[56:59], v[52:55], v[136:139], v[56:59]\n"
    "ds_read_b64_tr_b16 v[26:27], v3 offset:6976\n"
    "v_mfma_f32_16x16x32_bf16 v[56:59], v[144:147], v[44:47], v[56:59]\n"
    "ds_read_b64_tr_b16 v[24:25], v3 offset:6208\n"
    "v_mfma_f32_16x16x32_bf16 v[56:59], v[84:87], v[64:67], v[56:59]\n"
    "ds_read_b64_tr_b16 v[78:79], v3 offset:13120\n"
    "v_mfma_f32_16x16x32_bf16 v[56:59], v[168:171], v[172:175], v[56:59]\n"
    "ds_read_b64_tr_b16 v[76:77], v3 offset:12352\n"
    "v_mfma_f32_16x16x32_bf16 v[188:191], v[52:55], v[100:103], v[188:191]\n"
    "v_mfma_f32_16x16x32_bf16 v[188:191], v[144:147], v[128:131], v[188:191]\n"
    "v_mfma_f32_16x16x32_bf16 v[188:191], v[84:87], v[152:155], v[188:191]\n"
    "ds_read_b64_tr_b16 v[234:235], v8 offset:7040\n"
    "v_mfma_f32_16x16x32_bf16 v[188:191], v[168:171], v[184:187], v[188:191]\n"
    "ds_read_b64_tr_b16 v[232:233], v8 offset:6272\n"
    "v_mfma_f32_16x16x32_bf16 v[192:195], v[52:55], v[64:67], v[192:195]\n"
    "ds_read_b64_tr_b16 v[250:251], v8 offset:13184\n"
    "v_mfma_f32_16x16x32_bf16 v[192:195], v[144:147], v[172:175], v[192:195]\n"
    "ds_read_b64_tr_b16 v[248:249], v8 offset:12416\n"
    "v_mfma_f32_16x16x32_bf16 v[192:195], v[84:87], v[216:219], v[192:195]\n"
    "ds_read_b64_tr_b16 v[106:107], v9 offset:7040\n"
    "v_mfma_f32_16x16x32_bf16 v[192:195], v[168:171], v[220:223], v[192:195]\n"
    "ds_read_b64_tr_b16 v[104:105], v9 offset:6272\n"
    "v_mfma_f32_16x16x32_bf16 v[200:203], v[52:55], v[152:155], v[200:203]\n"
    "ds_read_b64_tr_b16 v[82:83], v9 offset:13184\n"
    "v_mfma_f32_16x16x32_bf16 v[200:203], v[144:147], v[184:187], v[200:203]\n"
    "ds_read_b64_tr_b16 v[80:81], v9 offset:12416\n"
    "s_waitcnt lgkmcnt(8)\n"
    "v_mfma_f32_16x16x32_bf16 v[224:227], v[68:71], v[136:139], v[224:227]\n"
    "ds_read_b64_tr_b16 v[118:119], v2 offset:7040\n"
    "v_mfma_f32_16x16x32_bf16 v[224:227], v[124:127], v[44:47], v[224:227]\n"
    "ds_read_b64_tr_b16 v[116:117], v2 offset:6272\n"
    "v_mfma_f32_16x16x32_bf16 v[224:227], v[24:27], v[64:67], v[224:227]\n"
    "ds_read_b64_tr_b16 v[98:99], v2 offset:13184\n"
    "v_mfma_f32_16x16x32_bf16 v[224:227], v[76:79], v[172:175], v[224:227]\n"
    "ds_read_b64_tr_b16 v[96:97], v2 offset:12416\n"
    "v_mfma_f32_16x16x32_bf16 v[160:163], v[68:71], v[100:103], v[160:163]\n"
    "ds_read_b64_tr_b16 v[90:91], v3 offset:7040\n"
    "v_mfma_f32_16x16x32_bf16 v[160:163], v[124:127], v[128:131], v[160:163]\n"
    "ds_read_b64_tr_b16 v[88:89], v3 offset:6272\n"
    "v_mfma_f32_16x16x32_bf16 v[160:163], v[24:27], v[152:155], v[160:163]\n"
    "ds_read_b64_tr_b16 v[238:239], v3 offset:13184\n"
    "v_mfma_f32_16x16x32_bf16 v[160:163], v[76:79], v[184:187], v[160:163]\n"
    "ds_read_b32 v1, v12 offset:56\n"
    "v_mfma_f32_16x16x32_bf16 v[204:207], v[68:71], v[64:67], v[204:207]\n"
    "ds_read_b64_tr_b16 v[236:237], v3 offset:12416\n"
    "v_mfma_f32_16x16x32_bf16 v[204:207], v[124:127], v[172:175], v[204:207]\n"
    "v_mfma_f32_16x16x32_bf16 v[204:207], v[24:27], v[216:219], v[204:207]\n"
    "v_mfma_f32_16x16x32_bf16 v[204:207], v[76:79], v[220:223], v[204:207]\n"
    "v_mfma_f32_16x16x32_bf16 v[36:39], v[68:71], v[152:155], v[36:39]\n"
    "v_mfma_f32_16x16x32_bf16 v[36:39], v[124:127], v[184:187], v[36:39]\n"
    "s_waitcnt lgkmcnt(5)\n"
    "v_mfma_f32_16x16x32_bf16 v[240:243], v[232:235], v[152:155], v[240:243]\n"
    "v_mfma_f32_16x16x32_bf16 v[240:243], v[248:251], v[184:187], v[240:243]\n"
    "v_mfma_f32_16x16x32_bf16 v[112:115], v[104:107], v[152:155], v[112:115]\n"
    "s_waitcnt lgkmcnt(1)\n"
    "v_cmp_ne_u32 vcc, 0, v1\n"
    "v_mfma_f32_16x16x32_bf16 v[112:115], v[80:83], v[184:187], v[112:115]\n"
    "v_add_u32 v8, 55296, v8\n"
    "v_add_u32 v17, 55296, v17\n"
    "v_mfma_f32_16x16x32_bf16 v[108:111], v[232:235], v[136:139], v[108:111]\n"
    "s_cbranch_vccnz LF_23\n"
    "LF_23b:\n"
    "v_mfma_f32_16x16x32_bf16 v[108:111], v[248:251], v[44:47], v[108:111]\n"
    "ds_read_b64_tr_b16 v[122:123], v8 offset:768\n"
    "v_add_u32 v2, 55296, v2\n"
    "v_mfma_f32_16x16x32_bf16 v[108:111], v[116:119], v[64:67], v[108:111]\n"
    "ds_read_b64_tr_b16 v[120:121], v8 offset:0\n"
    "v_add_u32 v3, 55296, v3\n"
    "v_mfma_f32_16x16x32_bf16 v[108:111], v[96:99], v[172:175], v[108:111]\n"
    "ds_read_b64_tr_b16 v[22:23], v8 offset:6912\n"
    "v_add_u32 v18, 55296, v18\n"
    "v_mfma_f32_16x16x32_bf16 v[92:95], v[104:107], v[136:139], v[92:95]\n"
    "ds_read_b64_tr_b16 v[20:21], v8 offset:6144\n"
    "v_mov_b32_dpp v139, v139 row_shl:1 row_mask:0xf bank_mask:0xf bound_ctrl:0\n"
    "v_mfma_f32_16x16x32_bf16 v[92:95], v[80:83], v[44:47], v[92:95]\n"
    "ds_read_b64_tr_b16 v[142:143], v8 offset:13056\n"
    "v_mov_b32_dpp v138, v138 row_shl:1 row_mask:0xf bank_mask:0xf bound_ctrl:0\n"
    "v_mfma_f32_16x16x32_bf16 v[92:95], v[88:91], v[64:67], v[92:95]\n"
    "ds_read_b64_tr_b16 v[140:141], v8 offset:12288\n"
    "s_waitcnt lgkmcnt(6)\n"
    "v_mfma_f32_16x16x32_bf16 v[92:95], v[236:239], v[172:175], v[92:95]\n"
    "ds_read_b64_tr_b16 v[30:31], v17 offset:768\n"
    "v_mov_b32_dpp v137, v137 row_shl:1 row_mask:0xf bank_mask:0xf bound_ctrl:0\n"
    "v_mfma_f32_16x16x32_bf16 v[208:211], v[232:235], v[100:103], v[208:211]\n"
    "ds_read_b64_tr_b16 v[28:29], v17 offset:0\n"
    "v_mov_b32_dpp v136, v136 row_shl:1 row_mask:0xf bank_mask:0xf bound_ctrl:0\n"
    "v_mfma_f32_16x16x32_bf16 v[208:211], v[248:251], v[128:131], v[208:211]\n"
    "ds_read_b64_tr_b16 v[86:87], v17 offset:6912\n"
    "v_mov_b32_dpp v47, v47 row_shl:1 row_mask:0xf bank_mask:0xf bound_ctrl:0\n"
    "v_mfma_f32_16x16x32_bf16 v[208:211], v[116:119], v[152:155], v[208:211]\n"
    "ds_read_b64_tr_b16 v[84:85], v17 offset:6144\n"
    "v_mov_b32_dpp v46, v46 row_shl:1 row_mask:0xf bank_mask:0xf bound_ctrl:0\n"
    "v_mfma_f32_16x16x32_bf16 v[208:211], v[96:99], v[184:187], v[208:211]\n"
    "ds_read_b64_tr_b16 v[170:171], v17 offset:13056\n"
    "v_mov_b32_dpp v45, v45 row_shl:1 row_mask:0xf bank_mask:0xf bound_ctrl:0\n"
    "v_mfma_f32_16x16x32_bf16 v[244:247], v[104:107], v[100:103], v[244:247]\n"
    "ds_read_b64_tr_b16 v[168:169], v17 offset:12288\n"
    "v_mov_b32_dpp v44, v44 row_shl:1 row_mask:0xf bank_mask:0xf bound_ctrl:0\n"
    "v_mfma_f32_16x16x32_bf16 v[244:247], v[80:83], v[128:131], v[244:247]\n"
    "ds_read_b64_tr_b16 v[54:55], v2 offset:768\n"
    "v_mov_b32_dpp v103, v103 row_shl:1 row_mask:0xf bank_mask:0xf bound_ctrl:0\n"
    "v_mfma_f32_16x16x32_bf16 v[244:247], v[88:91], v[152:155], v[244:247]\n"
    "ds_read_b64_tr_b16 v[52:53], v2 offset:0\n"
    "v_mov_b32_dpp v102, v102 row_shl:1 row_mask:0xf bank_mask:0xf bound_ctrl:0\n"
    "v_mfma_f32_16x16x32_bf16 v[244:247], v[236:239], v[184:187], v[244:247]\n"
    "ds_read_b64_tr_b16 v[146:147], v2 offset:6912\n"
    "v_mov_b32_dpp v101, v101 row_shl:1 row_mask:0xf bank_mask:0xf bound_ctrl:0\n"
    "v_mfma_f32_16x16x32_bf16 v[180:183], v[232:235], v[64:67], v[180:183]\n"
    "ds_read_b64_tr_b16 v[144:145], v2 offset:6144\n"
    "v_mov_b32_dpp v100, v100 row_shl:1 row_mask:0xf bank_mask:0xf bound_ctrl:0\n"
    "v_mfma_f32_16x16x32_bf16 v[180:183], v[248:251], v[172:175], v[180:183]\n"
    "ds_read_b64_tr_b16 v[26:27], v2 offset:13056\n"
    "v_mov_b32_dpp v131, v131 row_shl:1 row_mask:0xf bank_mask:0xf bound_ctrl:0\n"
    "v_mfma_f32_16x16x32_bf16 v[180:183], v[116:119], v[216:219], v[180:183]\n"
    "ds_read_b64_tr_b16 v[24:25], v2 offset:12288\n"
    "v_mov_b32_dpp v130, v130 row_shl:1 row_mask:0xf bank_mask:0xf bound_ctrl:0\n"
    "v_mfma_f32_16x16x32_bf16 v[180:183], v[96:99], v[220:223], v[180:183]\n"
    "v_mov_b32_dpp v129, v129 row_shl:1 row_mask:0xf bank_mask:0xf bound_ctrl:0\n"
    "v_mov_b32_dpp v128, v128 row_shl:1 row_mask:0xf bank_mask:0xf bound_ctrl:0\n"
    "v_mfma_f32_16x16x32_bf16 v[252:255], v[104:107], v[64:67], v[252:255]\n"
    "v_mov_b32_dpp v67, v67 row_shl:1 row_mask:0xf bank_mask:0xf bound_ctrl:0\n"
    "v_mov_b32_dpp v66, v66 row_shl:1 row_mask:0xf bank_mask:0xf bound_ctrl:0\n"
    "v_mfma_f32_16x16x32_bf16 v[252:255], v[80:83], v[172:175], v[252:255]\n"
    "v_mov_b32_dpp v65, v65 row_shl:1 row_mask:0xf bank_mask:0xf bound_ctrl:0\n"
    "v_mov_b32_dpp v64, v64 row_shl:1 row_mask:0xf bank_mask:0xf bound_ctrl:0\n"
    "v_mfma_f32_16x16x32_bf16 v[252:255], v[88:91], v[216:219], v[252:255]\n"
    "v_mov_b32_dpp v175, v175 row_shl:1 row_mask:0xf bank_mask:0xf bound_ctrl:0\n"
    "v_mov_b32_dpp v174, v174 row_shl:1 row_mask:0xf bank_mask:0xf bound_ctrl:0\n"
    "v_mfma_f32_16x16x32_bf16 v[252:255], v[236:239], v[220:223], v[252:255]\n"
    "s_barrier\n"
    "s_waitcnt lgkmcnt(5)\n"
    "v_mfma_f32_16x16x32_bf16 v[132:135], v[120:123], v[48:51], v[132:135]\n"
    "v_mov_b32_dpp v173, v173 row_shl:1 row_mask:0xf bank_mask:0xf bound_ctrl:0\n"
    "v_mov_b32_dpp v172, v172 row_shl:1 row_mask:0xf bank_mask:0xf bound_ctrl:0\n"
    "v_mfma_f32_16x16x32_bf16 v[132:135], v[20:23], v[136:139], v[132:135]\n"
    "v_mov_b32_dpp v155, v155 row_shl:1 row_mask:0xf bank_mask:0xf bound_ctrl:0\n"
    "v_mov_b32_dpp v154, v154 row_shl:1 row_mask:0xf bank_mask:0xf bound_ctrl:0\n"
    "v_mfma_f32_16x16x32_bf16 v[132:135], v[140:143], v[44:47], v[132:135]\n"
    "v_mov_b32_dpp v153, v153 row_shl:1 row_mask:0xf bank_mask:0xf bound_ctrl:0\n"
    "v_mov_b32_dpp v152, v152 row_shl:1 row_mask:0xf bank_mask:0xf bound_ctrl:0\n"
    "v_mfma_f32_16x16x32_bf16 v[132:135], v[28:31], v[196:199], v[132:135]\n"
    "v_add_u32 v9, 55296, v9\n"
    "v_mov_b32_dpp v187, v187 row_shl:1 row_mask:0xf bank_mask:0xf bound_ctrl:0\n"
    "v_mfma_f32_16x16x32_bf16 v[132:135], v[84:87], v[100:103], v[132:135]\n"
    "v_mov_b32_dpp v186, v186 row_shl:1 row_mask:0xf bank_mask:0xf bound_ctrl:0\n"
    "v_mov_b32_dpp v185, v185 row_shl:1 row_mask:0xf bank_mask:0xf bound_ctrl:0\n"
    "v_mfma_f32_16x16x32_bf16 v[132:135], v[168:171], v[128:131], v[132:135]\n"
    "v_mov_b32_dpp v184, v184 row_shl:1 row_mask:0xf bank_mask:0xf bound_ctrl:0\n"
    "s_waitcnt lgkmcnt(0)\n"
    "v_mfma_f32_16x16x32_bf16 v[132:135], v[52:55], v[40:43], v[132:135]\n"
    "v_mov_b32_dpp v219, v219 row_shl:1 row_mask:0xf bank_mask:0xf bound_ctrl:0\n"
    "v_mov_b32_dpp v218, v218 row_shl:1 row_mask:0xf bank_mask:0xf bound_ctrl:0\n"
    "v_mfma_f32_16x16x32_bf16 v[132:135], v[144:147], v[64:67], v[132:135]\n"
    "v_mov_b32_dpp v217, v217 row_shl:1 row_mask:0xf bank_mask:0xf bound_ctrl:0\n"
    "v_mov_b32_dpp v216, v216 row_shl:1 row_mask:0xf bank_mask:0xf bound_ctrl:0\n"
    "v_mfma_f32_16x16x32_bf16 v[132:135], v[24:27], v[172:175], v[132:135]\n"
    "v_mov_b32_dpp v223, v223 row_shl:1 row_mask:0xf bank_mask:0xf bound_ctrl:0\n"
    "v_mov_b32_dpp v222, v222 row_shl:1 row_mask:0xf bank_mask:0xf bound_ctrl:0\n"
    "v_mfma_f32_16x16x32_bf16 v[60:63], v[120:123], v[196:199], v[60:63]\n"
    "v_mov_b32_dpp v221, v221 row_shl:1 row_mask:0xf bank_mask:0xf bound_ctrl:0\n"
    "v_mov_b32_dpp v220, v220 row_shl:1 row_mask:0xf bank_mask:0xf bound_ctrl:0\n"
    "v_mfma_f32_16x16x32_bf16 v[60:63], v[20:23], v[100:103], v[60:63]\n"
    "ds_read_b64_tr_b16 v[78:79], v9 offset:768\n"
    "v_mfma_f32_16x16x32_bf16 v[60:63], v[140:143], v[128:131], v[60:63]\n"
    "ds_read_b64_tr_b16 v[76:77], v9 offset:0\n"
    "v_mfma_f32_16x16x32_bf16 v[60:63], v[28:31], v[40:43], v[60:63]\n"
    "ds_read_b64_tr_b16 v[70:71], v9 offset:6912\n"
    "v_mfma_f32_16x16x32_bf16 v[60:63], v[84:87], v[64:67], v[60:63]\n"
    "ds_read_b64_tr_b16 v[68:69], v9 offset:6144\n"
    "v_mfma_f32_16x16x32_bf16 v[60:63], v[168:171], v[172:175], v[60:63]\n"
    "LPT1:\n"
    "s_cbranch_execz LCS\n"
    "ds_read_b64_tr_b16 v[126:127], v9 offset:13056\n"
    "v_mfma_f32_16x16x32_bf16 v[60:63], v[52:55], v[148:151], v[60:63]\n"
    "ds_read_b64_tr_b16 v[124:125], v9 offset:12288\n"
    "v_mfma_f32_16x16x32_bf16 v[60:63], v[144:147], v[152:155], v[60:63]\n"
    "ds_read_b64_tr_b16 v[234:235], v18 offset:768\n"
    "v_mfma_f32_16x16x32_bf16 v[60:63], v[24:27], v[184:187], v[60:63]\n"
    "ds_read_b64_tr_b16 v[232:233], v18 offset:0\n"
    "v_mfma_f32_16x16x32_bf16 v[72:75], v[120:123], v[40:43], v[72:75]\n"
    "ds_read_b64_tr_b16 v[250:251], v18 offset:6912\n"
    "v_mfma_f32_16x16x32_bf16 v[72:75], v[20:23], v[64:67], v[72:75]\n"
    "ds_read_b64_tr_b16 v[248:249], v18 offset:6144\n"
    "v_mfma_f32_16x16x32_bf16 v[72:75], v[140:143], v[172:175], v[72:75]\n"
    "ds_read_b64_tr_b16 v[118:119], v18 offset:13056\n"
    "v_mfma_f32_16x16x32_bf16 v[72:75], v[28:31], v[148:151], v[72:75]\n"
    "ds_read_b64_tr_b16 v[116:117], v18 offset:12288\n"
    "v_mfma_f32_16x16x32_bf16 v[72:75], v[84:87], v[152:155], v[72:75]\n"
    "ds_read_b64_tr_b16 v[98:99], v3 offset:768\n"
    "v_mfma_f32_16x16x32_bf16 v[72:75], v[168:171], v[184:187], v[72:75]\n"
    "ds_read_b64_tr_b16 v[96:97], v3 offset:0\n"
    "v_mfma_f32_16x16x32_bf16 v[72:75], v[52:55], v[212:215], v[72:75]\n"
    "ds_read_b64_tr_b16 v[106:107], v3 offset:6912\n"
    "v_mfma_f32_16x16x32_bf16 v[72:75], v[144:147], v[216:219], v[72:75]\n"
    "ds_read_b64_tr_b16 v[104:105], v3 offset:6144\n"
    "v_mfma_f32_16x16x32_bf16 v[72:75], v[24:27], v[220:223], v[72:75]\n"
    "ds_read_b64_tr_b16 v[82:83], v3 offset:13056\n"
    "v_mfma_f32_16x16x32_bf16 v[156:159], v[120:123], v[148:151], v[156:159]\n"
    "ds_read_b64_tr_b16 v[80:81], v3 offset:12288\n"
    "v_mfma_f32_16x16x32_bf16 v[156:159], v[20:23], v[152:155], v[156:159]\n"
    "v_mfma_f32_16x16x32_bf16 v[156:159], v[140:143], v[184:187], v[156:159]\n"
    "v_mfma_f32_16x16x32_bf16 v[156:159], v[28:31], v[212:215], v[156:159]\n"
    "v_mfma_f32_16x16x32_bf16 v[156:159], v[84:87], v[216:219], v[156:159]\n"
    "v_mfma_f32_16x16x32_bf16 v[156:159], v[168:171], v[220:223], v[156:159]\n"
    "s_waitcnt lgkmcnt(5)\n"
    "v_mfma_f32_16x16x32_bf16 v[228:231], v[76:79], v[48:51], v[228:231]\n"
    "v_mfma_f32_16x16x32_bf16 v[228:231], v[68:71], v[136:139], v[228:231]\n"
    "v_mfma_f32_16x16x32_bf16 v[228:231], v[124:127], v[44:47], v[228:231]\n"
    "v_mfma_f32_16x16x32_bf16 v[228:231], v[232:235], v[196:199], v[228:231]\n"
    "v_mfma_f32_16x16x32_bf16 v[228:231], v[248:251], v[100:103], v[228:231]\n"
    "v_mfma_f32_16x16x32_bf16 v[228:231], v[116:119], v[128:131], v[228:231]\n"
    "s_waitcnt lgkmcnt(0)\n"
    "v_mfma_f32_16x16x32_bf16 v[228:231], v[96:99], v[40:43], v[228:231]\n"
    "v_mfma_f32_16x16x32_bf16 v[228:231], v[104:107], v[64:67], v[228:231]\n"
    "ds_read_b64_tr_b16 v[90:91], v8 offset:832\n"
    "v_mfma_f32_16x16x32_bf16 v[228:231], v[80:83], v[172:175], v[228:231]\n"
    "ds_read_b64_tr_b16 v[88:89], v8 offset:64\n"
    "v_mfma_f32_16x16x32_bf16 v[164:167], v[76:79], v[196:199], v[164:167]\n"
    "ds_read_b64_tr_b16 v[238:239], v8 offset:6976\n"
    "v_mfma_f32_16x16x32_bf16 v[164:167], v[68:71], v[100:103], v[164:167]\n"
    "ds_read_b64_tr_b16 v[236:237], v8 offset:6208\n"
    "v_mfma_f32_16x16x32_bf16 v[164:167], v[124:127], v[128:131], v[164:167]\n"
    "ds_read_b64_tr_b16 v[54:55], v8 offset:13120\n"
    "v_mfma_f32_16x16x32_bf16 v[164:167], v[232:235], v[40:43], v[164:167]\n"
    "ds_read_b64_tr_b16 v[52:53], v8 offset:12352\n"
    "v_mfma_f32_16x16x32_bf16 v[164:167], v[248:251], v[64:67], v[164:167]\n"
    "ds_read_b64_tr_b16 v[146:147], v17 offset:832\n"
    "v_mfma_f32_16x16x32_bf16 v[164:167], v[116:119], v[172:175], v[164:167]\n"
    "ds_read_b64_tr_b16 v[144:145], v17 offset:64\n"
    "v_mfma_f32_16x16x32_bf16 v[164:167], v[96:99], v[148:151], v[164:167]\n"
    "ds_read_b64_tr_b16 v[26:27], v17 offset:6976\n"
    "v_mfma_f32_16x16x32_bf16 v[164:167], v[104:107], v[152:155], v[164:167]\n"
    "ds_read_b64_tr_b16 v[24:25], v17 offset:6208\n"
    "v_mfma_f32_16x16x32_bf16 v[164:167], v[80:83], v[184:187], v[164:167]\n"
    "ds_read_b64_tr_b16 v[122:123], v17 offset:13120\n"
    "v_mfma_f32_16x16x32_bf16 v[176:179], v[76:79], v[40:43], v[176:179]\n"
    "ds_read_b64_tr_b16 v[120:121], v17 offset:12352\n"
    "v_mfma_f32_16x16x32_bf16 v[176:179], v[68:71], v[64:67], v[176:179]\n"
    "ds_read_b64_tr_b16 v[22:23], v2 offset:832\n"
    "v_mfma_f32_16x16x32_bf16 v[176:179], v[124:127], v[172:175], v[176:179]\n"
    "ds_read_b64_tr_b16 v[20:21], v2 offset:64\n"
    "v_mfma_f32_16x16x32_bf16 v[176:179], v[232:235], v[148:151], v[176:179]\n"
    "ds_read_b64_tr_b16 v[142:143], v2 offset:6976\n"
    "v_mfma_f32_16x16x32_bf16 v[176:179], v[248:251], v[152:155], v[176:179]\n"
    "ds_read_b64_tr_b16 v[140:141], v2 offset:6208\n"
    "v_mfma_f32_16x16x32_bf16 v[176:179], v[116:119], v[184:187], v[176:179]\n"
    "ds_read_b64_tr_b16 v[30:31], v2 offset:13120\n"
    "v_mfma_f32_16x16x32_bf16 v[176:179], v[96:99], v[212:215], v[176:179]\n"
    "ds_read_b64_tr_b16 v[28:29], v2 offset:12352\n"
    "v_mfma_f32_16x16x32_bf16 v[176:179], v[104:107], v[216:219], v[176:179]\n"
    "v_mfma_f32_16x16x32_bf16 v[176:179], v[80:83], v[220:223], v[176:179]\n"
    "v_mfma_f32_16x16x32_bf16 v[32:35], v[76:79], v[148:151], v[32:35]\n"
    "v_mfma_f32_16x16x32_bf16 v[32:35], v[68:71], v[152:155], v[32:35]\n"
    "v_mfma_f32_16x16x32_bf16 v[32:35], v[124:127], v[184:187], v[32:35]\n"
    "s_waitcnt lgkmcnt(5)\n"
    "v_mfma_f32_16x16x32_bf16 v[56:59], v[88:91], v[48:51], v[56:59]\n"
    "v_mfma_f32_16x16x32_bf16 v[56:59], v[236:239], v[136:139], v[56:59]\n"
    "v_mfma_f32_16x16x32_bf16 v[56:59], v[52:55], v[44:47], v[56:59]\n"
    "v_mfma_f32_16x16x32_bf16 v[56:59], v[144:147], v[196:199], v[56:59]\n"
    "v_mfma_f32_16x16x32_bf16 v[56:59], v[24:27], v[100:103], v[56:59]\n"
    "v_mfma_f32_16x16x32_bf16 v[56:59], v[120:123], v[128:131], v[56:59]\n"
    "s_waitcnt lgkmcnt(0)\n"
    "v_mfma_f32_16x16x32_bf16 v[56:59], v[20:23], v[40:43], v[56:59]\n"
    "v_mfma_f32_16x16x32_bf16 v[56:59], v[140:143], v[64:67], v[56:59]\n"
    "v_mfma_f32_16x16x32_bf16 v[56:59], v[28:31], v[172:175], v[56:59]\n"
    "v_mfma_f32_16x16x32_bf16 v[188:191], v[88:91], v[196:199], v[188:191]\n"
    "v_mfma_f32_16x16x32_bf16 v[188:191], v[236:239], v[100:103], v[188:191]\n"
    "ds_read_b64_tr_b16 v[86:87], v9 offset:832\n"
    "v_mfma_f32_16x16x32_bf16 v[188:191], v[52:55], v[128:131], v[188:191]\n"
    "ds_read_b64_tr_b16 v[84:85], v9 offset:64\n"
    "v_mfma_f32_16x16x32_bf16 v[188:191], v[144:147], v[40:43], v[188:191]\n"
    "ds_read_b64_tr_b16 v[170:171], v9 offset:6976\n"
    "v_mfma_f32_16x16x32_bf16 v[188:191], v[24:27], v[64:67], v[188:191]\n"
    "ds_read_b64_tr_b16 v[168:169], v9 offset:6208\n"
    "v_mfma_f32_16x16x32_bf16 v[188:191], v[120:123], v[172:175], v[188:191]\n"
    "ds_read_b64_tr_b16 v[234:235], v9 offset:13120\n"
    "v_mfma_f32_16x16x32_bf16 v[188:191], v[20:23], v[148:151], v[188:191]\n"
    "ds_read_b64_tr_b16 v[232:233], v9 offset:12352\n"
    "v_mfma_f32_16x16x32_bf16 v[188:191], v[140:143], v[152:155], v[188:191]\n"
    "ds_read_b64_tr_b16 v[250:251], v18 offset:832\n"
    "v_mfma_f32_16x16x32_bf16 v[188:191], v[28:31], v[184:187], v[188:191]\n"
    "ds_read_b64_tr_b16 v[248:249], v18 offset:64\n"
    "v_mfma_f32_16x16x32_bf16 v[192:195], v[88:91], v[40:43], v[192:195]\n"
    "ds_read_b64_tr_b16 v[118:119], v18 offset:6976\n"
    "v_mfma_f32_16x16x32_bf16 v[192:195], v[236:239], v[64:67], v[192:195]\n"
    "ds_read_b64_tr_b16 v[116:117], v18 offset:6208\n"
    "v_mfma_f32_16x16x32_bf16 v[192:195], v[52:55], v[172:175], v[192:195]\n"
    "ds_read_b64_tr_b16 v[98:99], v18 offset:13120\n"
    "v_mfma_f32_16x16x32_bf16 v[192:195], v[144:147], v[148:151], v[192:195]\n"
    "ds_read_b64_tr_b16 v[96:97], v18 offset:12352\n"
    "v_mfma_f32_16x16x32_bf16 v[192:195], v[24:27], v[152:155], v[192:195]\n"
    "ds_read_b64_tr_b16 v[106:107], v3 offset:832\n"
    "v_mfma_f32_16x16x32_bf16 v[192:195], v[120:123], v[184:187], v[192:195]\n"
    "ds_read_b64_tr_b16 v[104:105], v3 offset:64\n"
    "v_mfma_f32_16x16x32_bf16 v[192:195], v[20:23], v[212:215], v[192:195]\n"
    "ds_read_b64_tr_b16 v[82:83], v3 offset:6976\n"
    "v_mfma_f32_16x16x32_bf16 v[192:195], v[140:143], v[216:219], v[192:195]\n"
    "ds_read_b64_tr_b16 v[80:81], v3 offset:6208\n"
    "v_mfma_f32_16x16x32_bf16 v[192:195], v[28:31], v[220:223], v[192:195]\n"
    "ds_read_b64_tr_b16 v[78:79], v3 offset:13120\n"
    "v_mfma_f32_16x16x32_bf16 v[200:203], v[88:91], v[148:151], v[200:203]\n"
    "ds_read_b64_tr_b16 v[76:77], v3 offset:12352\n"
    "v_mfma_f32_16x16x32_bf16 v[200:203], v[236:239], v[152:155], v[200:203]\n"
    "v_mfma_f32_16x16x32_bf16 v[200:203], v[52:55], v[184:187], v[200:203]\n"
    "v_mfma_f32_16x16x32_bf16 v[200:203], v[144:147], v[212:215], v[200:203]\n"
    "v_mfma_f32_16x16x32_bf16 v[200:203], v[24:27], v[216:219], v[200:203]\n"
    "v_mfma_f32_16x16x32_bf16 v[200:203], v[120:123], v[220:223], v[200:203]\n"
    "s_waitcnt lgkmcnt(5)\n"
    "v_mfma_f32_16x16x32_bf16 v[224:227], v[84:87], v[48:51], v[224:227]\n"
    "v_mfma_f32_16x16x32_bf16 v[224:227], v[168:171], v[136:139], v[224:227]\n"
    "v_mfma_f32_16x16x32_bf16 v[224:227], v[232:235], v[44:47], v[224:227]\n"
    "v_mfma_f32_16x16x32_bf16 v[224:227], v[248:251], v[196:199], v[224:227]\n"
    "v_mfma_f32_16x16x32_bf16 v[224:227], v[116:119], v[100:103], v[224:227]\n"
    "v_mfma_f32_16x16x32_bf16 v[224:227], v[96:99], v[128:131], v[224:227]\n"
    "s_waitcnt lgkmcnt(0)\n"
    "v_mfma_f32_16x16x32_bf16 v[224:227], v[104:107], v[40:43], v[224:227]\n"
    "v_mfma_f32_16x16x32_bf16 v[224:227], v[80:83], v[64:67], v[224:227]\n"
    "ds_read_b64_tr_b16 v[70:71], v8 offset:896\n"
    "v_mfma_f32_16x16x32_bf16 v[224:227], v[76:79], v[172:175], v[224:227]\n"
    "ds_read_b64_tr_b16 v[68:69], v8 offset:128\n"
    "v_mfma_f32_16x16x32_bf16 v[160:163], v[84:87], v[196:199], v[160:163]\n"
    "ds_read_b64_tr_b16 v[126:127], v8 offset:7040\n"
    "v_mfma_f32_16x16x32_bf16 v[160:163], v[168:171], v[100:103], v[160:163]\n"
    "ds_read_b64_tr_b16 v[124:125], v8 offset:6272\n"
    "v_mfma_f32_16x16x32_bf16 v[160:163], v[232:235], v[128:131], v[160:163]\n"
    "ds_read_b64_tr_b16 v[22:23], v8 offset:13184\n"
    "v_mfma_f32_16x16x32_bf16 v[160:163], v[248:251], v[40:43], v[160:163]\n"
    "ds_read_b64_tr_b16 v[20:21], v8 offset:12416\n"
    "v_mfma_f32_16x16x32_bf16 v[160:163], v[116:119], v[64:67], v[160:163]\n"
    "ds_read_b64_tr_b16 v[142:143], v17 offset:896\n"
    "v_mfma_f32_16x16x32_bf16 v[160:163], v[96:99], v[172:175], v[160:163]\n"
    "ds_read_b64_tr_b16 v[140:141], v17 offset:128\n"
    "v_mfma_f32_16x16x32_bf16 v[160:163], v[104:107], v[148:151], v[160:163]\n"
    "ds_read_b64_tr_b16 v[30:31], v17 offset:7040\n"
    "v_mfma_f32_16x16x32_bf16 v[160:163], v[80:83], v[152:155], v[160:163]\n"
    "ds_read_b64_tr_b16 v[28:29], v17 offset:6272\n"
    "v_mfma_f32_16x16x32_bf16 v[160:163], v[76:79], v[184:187], v[160:163]\n"
    "ds_read_b64_tr_b16 v[90:91], v17 offset:13184\n"
    "v_mfma_f32_16x16x32_bf16 v[204:207], v[84:87], v[40:43], v[204:207]\n"
    "ds_read_b64_tr_b16 v[88:89], v17 offset:12416\n"
    "v_mfma_f32_16x16x32_bf16 v[204:207], v[168:171], v[64:67], v[204:207]\n"
    "ds_read_b64_tr_b16 v[238:239], v2 offset:896\n"
    "v_mfma_f32_16x16x32_bf16 v[204:207], v[232:235], v[172:175], v[204:207]\n"
    "ds_read_b64_tr_b16 v[236:237], v2 offset:128\n"
    "v_mfma_f32_16x16x32_bf16 v[204:207], v[248:251], v[148:151], v[204:207]\n"
    "ds_read_b64_tr_b16 v[54:55], v2 offset:7040\n"
    "v_mfma_f32_16x16x32_bf16 v[204:207], v[116:119], v[152:155], v[204:207]\n"
    "ds_read_b64_tr_b16 v[52:53], v2 offset:6272\n"
    "v_mfma_f32_16x16x32_bf16 v[204:207], v[96:99], v[184:187], v[204:207]\n"
    "ds_read_b64_tr_b16 v[146:147], v2 offset:13184\n"
    "v_mfma_f32_16x16x32_bf16 v[204:207], v[104:107], v[212:215], v[204:207]\n"
    "ds_read_b64_tr_b16 v[144:145], v2 offset:12416\n"
    "v_mfma_f32_16x16x32_bf16 v[204:207], v[80:83], v[216:219], v[204:207]\n"
    "v_mfma_f32_16x16x32_bf16 v[204:207], v[76:79], v[220:223], v[204:207]\n"
    "v_mfma_f32_16x16x32_bf16 v[36:39], v[84:87], v[148:151], v[36:39]\n"
    "v_mfma_f32_16x16x32_bf16 v[36:39], v[168:171], v[152:155], v[36:39]\n"
    "v_mfma_f32_16x16x32_bf16 v[36:39], v[232:235], v[184:187], v[36:39]\n"
    "s_waitcnt lgkmcnt(5)\n"
    "v_mfma_f32_16x16x32_bf16 v[108:111], v[68:71], v[48:51], v[108:111]\n"
    "v_mfma_f32_16x16x32_bf16 v[108:111], v[124:127], v[136:139], v[108:111]\n"
    "v_mfma_f32_16x16x32_bf16 v[108:111], v[20:23], v[44:47], v[108:111]\n"
    "v_mfma_f32_16x16x32_bf16 v[108:111], v[140:143], v[196:199], v[108:111]\n"
    "v_mfma_f32_16x16x32_bf16 v[108:111], v[28:31], v[100:103], v[108:111]\n"
    "v_mfma_f32_16x16x32_bf16 v[108:111], v[88:91], v[128:131], v[108:111]\n"
    "s_waitcnt lgkmcnt(0)\n"
    "v_mfma_f32_16x16x32_bf16 v[108:111], v[236:239], v[40:43], v[108:111]\n"
    "v_mfma_f32_16x16x32_bf16 v[108:111], v[52:55], v[64:67], v[108:111]\n"
    "v_mfma_f32_16x16x32_bf16 v[108:111], v[144:147], v[172:175], v[108:111]\n"
    "v_mfma_f32_16x16x32_bf16 v[208:211], v[68:71], v[196:199], v[208:211]\n"
    "v_mfma_f32_16x16x32_bf16 v[208:211], v[124:127], v[100:103], v[208:211]\n"
    "ds_read_b64_tr_b16 v[26:27], v9 offset:896\n"
    "v_mfma_f32_16x16x32_bf16 v[208:211], v[20:23], v[128:131], v[208:211]\n"
    "ds_read_b64_tr_b16 v[24:25], v9 offset:128\n"
    "v_mfma_f32_16x16x32_bf16 v[208:211], v[140:143], v[40:43], v[208:211]\n"
    "ds_read_b64_tr_b16 v[122:123], v9 offset:7040\n"
    "v_mfma_f32_16x16x32_bf16 v[208:211], v[28:31], v[64:67], v[208:211]\n"
    "ds_read_b64_tr_b16 v[120:121], v9 offset:6272\n"
    "v_mfma_f32_16x16x32_bf16 v[208:211], v[88:91], v[172:175], v[208:211]\n"
    "ds_read_b64_tr_b16 v[250:251], v9 offset:13184\n"
    "v_mfma_f32_16x16x32_bf16 v[208:211], v[236:239], v[148:151], v[208:211]\n"
    "ds_read_b64_tr_b16 v[248:249], v9 offset:12416\n"
    "v_mfma_f32_16x16x32_bf16 v[208:211], v[52:55], v[152:155], v[208:211]\n"
    "ds_read_b64_tr_b16 v[118:119], v18 offset:896\n"
    "v_mfma_f32_16x16x32_bf16 v[208:211], v[144:147], v[184:187], v[208:211]\n"
    "ds_read_b64_tr_b16 v[116:117], v18 offset:128\n"
    "v_mfma_f32_16x16x32_bf16 v[180:183], v[68:71], v[40:43], v[180:183]\n"
    "ds_read_b64_tr_b16 v[98:99], v18 offset:7040\n"
    "v_mfma_f32_16x16x32_bf16 v[180:183], v[124:127], v[64:67], v[180:183]\n"
    "ds_read_b64_tr_b16 v[96:97], v18 offset:6272\n"
    "v_mfma_f32_16x16x32_bf16 v[180:183], v[20:23], v[172:175], v[180:183]\n"
    "ds_read_b64_tr_b16 v[106:107], v18 offset:13184\n"
    "v_mfma_f32_16x16x32_bf16 v[180:183], v[140:143], v[148:151], v[180:183]\n"
    "ds_read_b64_tr_b16 v[104:105], v18 offset:12416\n"
    "v_mfma_f32_16x16x32_bf16 v[180:183], v[28:31], v[152:155], v[180:183]\n"
    "ds_read_b64_tr_b16 v[82:83], v3 offset:896\n"
    "v_mfma_f32_16x16x32_bf16 v[180:183], v[88:91], v[184:187], v[180:183]\n"
    "ds_read_b64_tr_b16 v[80:81], v3 offset:128\n"
    "v_mfma_f32_16x16x32_bf16 v[180:183], v[236:239], v[212:215], v[180:183]\n"
    "ds_read_b64_tr_b16 v[78:79], v3 offset:7040\n"
    "v_mfma_f32_16x16x32_bf16 v[180:183], v[52:55], v[216:219], v[180:183]\n"
    "ds_read_b64_tr_b16 v[76:77], v3 offset:6272\n"
    "v_mfma_f32_16x16x32_bf16 v[180:183], v[144:147], v[220:223], v[180:183]\n"
    "ds_read_b32 v15, v12 offset:64\n"
    "v_mfma_f32_16x16x32_bf16 v[240:243], v[68:71], v[148:151], v[240:243]\n"
    "ds_read_b64_tr_b16 v[86:87], v3 offset:13184\n"
    "v_mfma_f32_16x16x32_bf16 v[240:243], v[124:127], v[152:155], v[240:243]\n"
    "ds_read_b64_tr_b16 v[84:85], v3 offset:12416\n"
    "v_mfma_f32_16x16x32_bf16 v[240:243], v[20:23], v[184:187], v[240:243]\n"
    "v_mfma_f32_16x16x32_bf16 v[240:243], v[140:143], v[212:215], v[240:243]\n"
    "s_mul_i32 s50, s10, 4294819840\n"
    "s_add_u32 s50, s50, 55296\n"
    "v_mfma_f32_16x16x32_bf16 v[240:243], v[28:31], v[216:219], v[240:243]\n"
    "v_mfma_f32_16x16x32_bf16 v[240:243], v[88:91], v[220:223], v[240:243]\n"
    "s_waitcnt lgkmcnt(6)\n"
    "v_mfma_f32_16x16x32_bf16 v[112:115], v[24:27], v[148:151], v[112:115]\n"
    "v_mfma_f32_16x16x32_bf16 v[112:115], v[120:123], v[152:155], v[112:115]\n"
    "v_mfma_f32_16x16x32_bf16 v[112:115], v[248:251], v[184:187], v[112:115]\n"
    "s_waitcnt lgkmcnt(2)\n"
    "v_cmp_ne_u32 vcc, 0, v15\n"
    "v_mfma_f32_16x16x32_bf16 v[92:95], v[24:27], v[48:51], v[92:95]\n"
    "v_mov_b32_dpp v51, v51 row_shl:1 row_mask:0xf bank_mask:0xf bound_ctrl:0\n"
    "v_mov_b32_dpp v50, v50 row_shl:1 row_mask:0xf bank_mask:0xf bound_ctrl:0\n"
    "v_mfma_f32_16x16x32_bf16 v[92:95], v[120:123], v[136:139], v[92:95]\n"
    "s_cbranch_vccnz LF_24\n"
    "LF_24b:\n"
    "v_mfma_f32_16x16x32_bf16 v[92:95], v[248:251], v[44:47], v[92:95]\n"
    "v_add_u32 v8, s50, v8\n"
    "v_add_u32 v17, 55296, v17\n"
    "v_mfma_f32_16x16x32_bf16 v[92:95], v[116:119], v[196:199], v[92:95]\n"
    "ds_read_b64_tr_b16 v[170:171], v8 offset:768\n"
    "v_add_u32 v9, s50, v9\n"
    "v_mfma_f32_16x16x32_bf16 v[92:95], v[96:99], v[100:103], v[92:95]\n"
    "ds_read_b64_tr_b16 v[168:169], v8 offset:0\n"
    "s_mul_i32 s50, s10, 147456\n"
    "s_add_u32 s50, s50, 4294875136\n"
    "v_mfma_f32_16x16x32_bf16 v[92:95], v[104:107], v[128:131], v[92:95]\n"
    "ds_read_b64_tr_b16 v[234:235], v8 offset:6912\n"
    "v_add_u32 v2, s50, v2\n"
    "v_mfma_f32_16x16x32_bf16 v[92:95], v[80:83], v[40:43], v[92:95]\n"
    "ds_read_b64_tr_b16 v[232:233], v8 offset:6144\n"
    "v_mov_b32_dpp v49, v49 row_shl:1 row_mask:0xf bank_mask:0xf bound_ctrl:0\n"
    "v_mfma_f32_16x16x32_bf16 v[92:95], v[76:79], v[64:67], v[92:95]\n"
    "ds_read_b64_tr_b16 v[238:239], v8 offset:13056\n"
    "s_waitcnt lgkmcnt(5)\n"
    "v_mfma_f32_16x16x32_bf16 v[92:95], v[84:87], v[172:175], v[92:95]\n"
    "ds_read_b64_tr_b16 v[236:237], v8 offset:12288\n"
    "v_mov_b32_dpp v48, v48 row_shl:1 row_mask:0xf bank_mask:0xf bound_ctrl:0\n"
    "v_mfma_f32_16x16x32_bf16 v[244:247], v[24:27], v[196:199], v[244:247]\n"
    "ds_read_b64_tr_b16 v[54:55], v17 offset:768\n"
    "v_mov_b32_dpp v139, v139 row_shl:1 row_mask:0xf bank_mask:0xf bound_ctrl:0\n"
    "v_mfma_f32_16x16x32_bf16 v[244:247], v[120:123], v[100:103], v[244:247]\n"
    "ds_read_b64_tr_b16 v[52:53], v17 offset:0\n"
    "v_mov_b32_dpp v138, v138 row_shl:1 row_mask:0xf bank_mask:0xf bound_ctrl:0\n"
    "v_mfma_f32_16x16x32_bf16 v[244:247], v[248:251], v[128:131], v[244:247]\n"
    "ds_read_b64_tr_b16 v[146:147], v17 offset:6912\n"
    "v_mov_b32_dpp v137, v137 row_shl:1 row_mask:0xf bank_mask:0xf bound_ctrl:0\n"
    "v_mfma_f32_16x16x32_bf16 v[244:247], v[116:119], v[40:43], v[244:247]\n"
    "ds_read_b64_tr_b16 v[144:145], v17 offset:6144\n"
    "v_mov_b32_dpp v136, v136 row_shl:1 row_mask:0xf bank_mask:0xf bound_ctrl:0\n"
    "v_mfma_f32_16x16x32_bf16 v[244:247], v[96:99], v[64:67], v[244:247]\n"
    "ds_read_b64_tr_b16 v[70:71], v17 offset:13056\n"
    "v_mov_b32_dpp v47, v47 row_shl:1 row_mask:0xf bank_mask:0xf bound_ctrl:0\n"
    "v_mfma_f32_16x16x32_bf16 v[244:247], v[104:107], v[172:175], v[244:247]\n"
    "ds_read_b64_tr_b16 v[68:69], v17 offset:12288\n"
    "v_mov_b32_dpp v46, v46 row_shl:1 row_mask:0xf bank_mask:0xf bound_ctrl:0\n"
    "v_mfma_f32_16x16x32_bf16 v[244:247], v[80:83], v[148:151], v[244:247]\n"
    "ds_read_b64_tr_b16 v[126:127], v2 offset:768\n"
    "v_mov_b32_dpp v45, v45 row_shl:1 row_mask:0xf bank_mask:0xf bound_ctrl:0\n"
    "v_mfma_f32_16x16x32_bf16 v[244:247], v[76:79], v[152:155], v[244:247]\n"
    "ds_read_b64_tr_b16 v[124:125], v2 offset:0\n"
    "v_mov_b32_dpp v44, v44 row_shl:1 row_mask:0xf bank_mask:0xf bound_ctrl:0\n"
    "v_mfma_f32_16x16x32_bf16 v[244:247], v[84:87], v[184:187], v[244:247]\n"
    "ds_read_b64_tr_b16 v[22:23], v2 offset:6912\n"
    "v_mov_b32_dpp v199, v199 row_shl:1 row_mask:0xf bank_mask:0xf bound_ctrl:0\n"
    "v_mfma_f32_16x16x32_bf16 v[252:255], v[24:27], v[40:43], v[252:255]\n"
    "v_mov_b32_dpp v198, v198 row_shl:1 row_mask:0xf bank_mask:0xf bound_ctrl:0\n"
    "v_mov_b32_dpp v197, v197 row_shl:1 row_mask:0xf bank_mask:0xf bound_ctrl:0\n"
    "v_mfma_f32_16x16x32_bf16 v[252:255], v[120:123], v[64:67], v[252:255]\n"
    "ds_read_b64_tr_b16 v[20:21], v2 offset:6144\n"
    "v_mov_b32_dpp v196, v196 row_shl:1 row_mask:0xf bank_mask:0xf bound_ctrl:0\n"
    "v_mfma_f32_16x16x32_bf16 v[252:255], v[248:251], v[172:175], v[252:255]\n"
    "ds_read_b64_tr_b16 v[142:143], v2 offset:13056\n"
    "v_mov_b32_dpp v103, v103 row_shl:1 row_mask:0xf bank_mask:0xf bound_ctrl:0\n"
    "v_mfma_f32_16x16x32_bf16 v[252:255], v[116:119], v[148:151], v[252:255]\n"
    "v_mov_b32_dpp v102, v102 row_shl:1 row_mask:0xf bank_mask:0xf bound_ctrl:0\n"
    "v_mov_b32_dpp v101, v101 row_shl:1 row_mask:0xf bank_mask:0xf bound_ctrl:0\n"
    "v_mfma_f32_16x16x32_bf16 v[252:255], v[96:99], v[152:155], v[252:255]\n"
    "ds_read_b64_tr_b16 v[140:141], v2 offset:12288\n"
    "v_mov_b32_dpp v100, v100 row_shl:1 row_mask:0xf bank_mask:0xf bound_ctrl:0\n"
    "v_mfma_f32_16x16x32_bf16 v[252:255], v[104:107], v[184:187], v[252:255]\n"
    "v_mov_b32_dpp v131, v131 row_shl:1 row_mask:0xf bank_mask:0xf bound_ctrl:0\n"
    "v_mov_b32_dpp v130, v130 row_shl:1 row_mask:0xf bank_mask:0xf bound_ctrl:0\n"
    "v_mfma_f32_16x16x32_bf16 v[252:255], v[80:83], v[212:215], v[252:255]\n"
    "v_mov_b32_dpp v129, v129 row_shl:1 row_mask:0xf bank_mask:0xf bound_ctrl:0\n"
    "v_mov_b32_dpp v128, v128 row_shl:1 row_mask:0xf bank_mask:0xf bound_ctrl:0\n"
    "v_mfma_f32_16x16x32_bf16 v[252:255], v[76:79], v[216:219], v[252:255]\n"
    "v_mov_b32_dpp v43, v43 row_shl:1 row_mask:0xf bank_mask:0xf bound_ctrl:0\n"
    "v_mov_b32_dpp v42, v42 row_shl:1 row_mask:0xf bank_mask:0xf bound_ctrl:0\n"
    "v_mfma_f32_16x16x32_bf16 v[252:255], v[84:87], v[220:223], v[252:255]\n"
    "v_mov_b32_dpp v41, v41 row_shl:1 row_mask:0xf bank_mask:0xf bound_ctrl:0\n"
    "s_waitcnt lgkmcnt(4)\n"
    "v_mfma_f32_16x16x32_bf16 v[132:135], v[168:171], v[48:51], v[132:135]\n"
    "v_mov_b32_dpp v40, v40 row_shl:1 row_mask:0xf bank_mask:0xf bound_ctrl:0\n"
    "v_mov_b32_dpp v67, v67 row_shl:1 row_mask:0xf bank_mask:0xf bound_ctrl:0\n"
    "v_mfma_f32_16x16x32_bf16 v[132:135], v[232:235], v[136:139], v[132:135]\n"
    "v_mov_b32_dpp v66, v66 row_shl:1 row_mask:0xf bank_mask:0xf bound_ctrl:0\n"
    "v_mov_b32_dpp v65, v65 row_shl:1 row_mask:0xf bank_mask:0xf bound_ctrl:0\n"
    "v_mfma_f32_16x16x32_bf16 v[132:135], v[236:239], v[44:47], v[132:135]\n"
    "v_mov_b32_dpp v64, v64 row_shl:1 row_mask:0xf bank_mask:0xf bound_ctrl:0\n"
    "v_mov_b32_dpp v175, v175 row_shl:1 row_mask:0xf bank_mask:0xf bound_ctrl:0\n"
    "v_mfma_f32_16x16x32_bf16 v[132:135], v[52:55], v[196:199], v[132:135]\n"
    "v_mov_b32_dpp v174, v174 row_shl:1 row_mask:0xf bank_mask:0xf bound_ctrl:0\n"
    "v_mov_b32_dpp v173, v173 row_shl:1 row_mask:0xf bank_mask:0xf bound_ctrl:0\n"
    "v_mfma_f32_16x16x32_bf16 v[132:135], v[144:147], v[100:103], v[132:135]\n"
    "v_mov_b32_dpp v172, v172 row_shl:1 row_mask:0xf bank_mask:0xf bound_ctrl:0\n"
    "v_mov_b32_dpp v151, v151 row_shl:1 row_mask:0xf bank_mask:0xf bound_ctrl:0\n"
    "v_mfma_f32_16x16x32_bf16 v[132:135], v[68:71], v[128:131], v[132:135]\n"
    "v_mov_b32_dpp v150, v150 row_shl:1 row_mask:0xf bank_mask:0xf bound_ctrl:0\n"
    "v_mov_b32_dpp v149, v149 row_shl:1 row_mask:0xf bank_mask:0xf bound_ctrl:0\n"
    "v_mfma_f32_16x16x32_bf16 v[132:135], v[124:127], v[40:43], v[132:135]\n"
    "v_mov_b32_dpp v148, v148 row_shl:1 row_mask:0xf bank_mask:0xf bound_ctrl:0\n"
    "s_waitcnt lgkmcnt(0)\n"
    "v_mfma_f32_16x16x32_bf16 v[132:135], v[20:23], v[64:67], v[132:135]\n"
    "v_mov_b32_dpp v155, v155 row_shl:1 row_mask:0xf bank_mask:0xf bound_ctrl:0\n"
    "v_mov_b32_dpp v154, v154 row_shl:1 row_mask:0xf bank_mask:0xf bound_ctrl:0\n"
    "v_mfma_f32_16x16x32_bf16 v[132:135], v[140:143], v[172:175], v[132:135]\n"
    "ds_read_b64_tr_b16 v[30:31], v9 offset:768\n"
    "v_mov_b32_dpp v153, v153 row_shl:1 row_mask:0xf bank_mask:0xf bound_ctrl:0\n"
    "v_mfma_f32_16x16x32_bf16 v[60:63], v[168:171], v[196:199], v[60:63]\n"
    "ds_read_b64_tr_b16 v[28:29], v9 offset:0\n"
    "v_mov_b32_dpp v152, v152 row_shl:1 row_mask:0xf bank_mask:0xf bound_ctrl:0\n"
    "v_mfma_f32_16x16x32_bf16 v[60:63], v[232:235], v[100:103], v[60:63]\n"
    "ds_read_b64_tr_b16 v[90:91], v9 offset:6912\n"
    "v_add_u32 v3, s50, v3\n"
    "v_mfma_f32_16x16x32_bf16 v[60:63], v[236:239], v[128:131], v[60:63]\n"
    "ds_read_b64_tr_b16 v[88:89], v9 offset:6144\n"
    "v_cvt_pk_bf16_f32 v132, v132, v133\n"
    "v_mfma_f32_16x16x32_bf16 v[60:63], v[52:55], v[40:43], v[60:63]\n"
    "v_cvt_pk_bf16_f32 v133, v134, v135\n"
    "ds_write_b64 v14, v[132:133] offset:0\n"
    "v_mfma_f32_16x16x32_bf16 v[60:63], v[144:147], v[64:67], v[60:63]\n"
    "v_add_u32 v18, 55296, v18\n"
    "v_mov_b32_dpp v187, v187 row_shl:1 row_mask:0xf bank_mask:0xf bound_ctrl:0\n"
    "v_mfma_f32_16x16x32_bf16 v[60:63], v[68:71], v[172:175], v[60:63]\n"
    "ds_read_b64_tr_b16 v[26:27], v9 offset:13056\n"
    "v_mov_b32_dpp v186, v186 row_shl:1 row_mask:0xf bank_mask:0xf bound_ctrl:0\n"
    "v_mfma_f32_16x16x32_bf16 v[60:63], v[124:127], v[148:151], v[60:63]\n"
    "ds_read_b64_tr_b16 v[24:25], v9 offset:12288\n"
    "v_mov_b32_dpp v185, v185 row_shl:1 row_mask:0xf bank_mask:0xf bound_ctrl:0\n"
    "v_mfma_f32_16x16x32_bf16 v[60:63], v[20:23], v[152:155], v[60:63]\n"
    "v_mov_b32_dpp v184, v184 row_shl:1 row_mask:0xf bank_mask:0xf bound_ctrl:0\n"
    "v_mov_b32_dpp v215, v215 row_shl:1 row_mask:0xf bank_mask:0xf bound_ctrl:0\n"
    "v_mov_b32_dpp v214, v214 row_shl:1 row_mask:0xf bank_mask:0xf bound_ctrl:0\n"
    "v_mfma_f32_16x16x32_bf16 v[60:63], v[140:143], v[184:187], v[60:63]\n"
    "ds_read_b64_tr_b16 v[122:123], v18 offset:768\n"
    "v_mov_b32_dpp v213, v213 row_shl:1 row_mask:0xf bank_mask:0xf bound_ctrl:0\n"
    "v_mfma_f32_16x16x32_bf16 v[72:75], v[168:171], v[40:43], v[72:75]\n"
    "ds_read_b64_tr_b16 v[120:121], v18 offset:0\n"
    "v_mov_b32_dpp v212, v212 row_shl:1 row_mask:0xf bank_mask:0xf bound_ctrl:0\n"
    "v_mfma_f32_16x16x32_bf16 v[72:75], v[232:235], v[64:67], v[72:75]\n"
    "ds_read_b64_tr_b16 v[250:251], v18 offset:6912\n"
    "v_mov_b32_dpp v219, v219 row_shl:1 row_mask:0xf bank_mask:0xf bound_ctrl:0\n"
    "v_mfma_f32_16x16x32_bf16 v[72:75], v[236:239], v[172:175], v[72:75]\n"
    "ds_read_b64_tr_b16 v[248:249], v18 offset:6144\n"
    "v_mov_b32_dpp v218, v218 row_shl:1 row_mask:0xf bank_mask:0xf bound_ctrl:0\n"
    "v_mfma_f32_16x16x32_bf16 v[72:75], v[52:55], v[148:151], v[72:75]\n"
    "ds_read_b64_tr_b16 v[118:119], v18 offset:13056\n"
    "v_mov_b32_dpp v217, v217 row_shl:1 row_mask:0xf bank_mask:0xf bound_ctrl:0\n"
    "v_mfma_f32_16x16x32_bf16 v[72:75], v[144:147], v[152:155], v[72:75]\n"
    "ds_read_b64_tr_b16 v[116:117], v18 offset:12288\n"
    "v_mov_b32_dpp v216, v216 row_shl:1 row_mask:0xf bank_mask:0xf bound_ctrl:0\n"
    "v_mfma_f32_16x16x32_bf16 v[72:75], v[68:71], v[184:187], v[72:75]\n"
    "ds_read_b64_tr_b16 v[98:99], v3 offset:768\n"
    "v_mov_b32_dpp v223, v223 row_shl:1 row_mask:0xf bank_mask:0xf bound_ctrl:0\n"
    "v_mfma_f32_16x16x32_bf16 v[72:75], v[124:127], v[212:215], v[72:75]\n"
    "ds_read_b64_tr_b16 v[96:97], v3 offset:0\n"
    "v_mov_b32_dpp v222, v222 row_shl:1 row_mask:0xf bank_mask:0xf bound_ctrl:0\n"
    "v_mfma_f32_16x16x32_bf16 v[72:75], v[20:23], v[216:219], v[72:75]\n"
    "v_mov_b32_dpp v221, v221 row_shl:1 row_mask:0xf bank_mask:0xf bound_ctrl:0\n"
    "v_mov_b32_dpp v220, v220 row_shl:1 row_mask:0xf bank_mask:0xf bound_ctrl:0\n"
    "ds_read_b64_tr_b16 v[106:107], v3 offset:6912\n"
    "s_nop 0\n"
    "v_mfma_f32_16x16x32_bf16 v[72:75], v[140:143], v[220:223], v[72:75]\n"
    "ds_read_b64_tr_b16 v[104:105], v3 offset:6144\n"
    "v_cvt_pk_bf16_f32 v60, v60, v61\n"
    "v_mfma_f32_16x16x32_bf16 v[156:159], v[168:171], v[148:151], v[156:159]\n"
    "ds_read_b64_tr_b16 v[82:83], v3 offset:13056\n"
    "v_cvt_pk_bf16_f32 v61, v62, v63\n"
    "v_mfma_f32_16x16x32_bf16 v[156:159], v[232:235], v[152:155], v[156:159]\n"
    "ds_read_b64_tr_b16 v[80:81], v3 offset:12288\n"
    "v_mfma_f32_16x16x32_bf16 v[156:159], v[236:239], v[184:187], v[156:159]\n"
    "ds_write_b64 v14, v[60:61] offset:1024\n"
    "v_mfma_f32_16x16x32_bf16 v[156:159], v[52:55], v[212:215], v[156:159]\n"
    "v_cvt_pk_bf16_f32 v72, v72, v73\n"
    "v_cvt_pk_bf16_f32 v73, v74, v75\n"
    "v_mfma_f32_16x16x32_bf16 v[156:159], v[144:147], v[216:219], v[156:159]\n"
    "ds_write_b64 v14, v[72:73] offset:2048\n"
    "v_mfma_f32_16x16x32_bf16 v[156:159], v[68:71], v[220:223], v[156:159]\n"
    "ds_read_b64_tr_b16 v[78:79], v8 offset:832\n"
    "s_waitcnt lgkmcnt(7)\n"
    "v_mfma_f32_16x16x32_bf16 v[228:231], v[28:31], v[48:51], v[228:231]\n"
    "ds_read_b64_tr_b16 v[76:77], v8 offset:64\n"
    "v_mfma_f32_16x16x32_bf16 v[228:231], v[88:91], v[136:139], v[228:231]\n"
    "ds_read_b64_tr_b16 v[86:87], v8 offset:6976\n"
    "v_mfma_f32_16x16x32_bf16 v[228:231], v[24:27], v[44:47], v[228:231]\n"
    "s_nop 0\n"
    "ds_write_b64 v13, v[158:159] offset:8\n"
    "v_mfma_f32_16x16x32_bf16 v[228:231], v[120:123], v[196:199], v[228:231]\n"
    "ds_write_b64 v13, v[156:157] offset:0\n"
    "v_mfma_f32_16x16x32_bf16 v[228:231], v[248:251], v[100:103], v[228:231]\n"
    "ds_read_b64_tr_b16 v[84:85], v8 offset:6208\n"
    "v_mfma_f32_16x16x32_bf16 v[228:231], v[116:119], v[128:131], v[228:231]\n"
    "ds_read_b64_tr_b16 v[134:135], v8 offset:13120\n"
    "v_mfma_f32_16x16x32_bf16 v[228:231], v[96:99], v[40:43], v[228:231]\n"
    "ds_read_b64_tr_b16 v[132:133], v8 offset:12352\n"
    "s_waitcnt lgkmcnt(9)\n"
    "v_mfma_f32_16x16x32_bf16 v[228:231], v[104:107], v[64:67], v[228:231]\n"
    "ds_read_b64_tr_b16 v[126:127], v17 offset:832\n"
    "v_mfma_f32_16x16x32_bf16 v[228:231], v[80:83], v[172:175], v[228:231]\n"
    "ds_read_b64_tr_b16 v[124:125], v17 offset:64\n"
    "v_mfma_f32_16x16x32_bf16 v[164:167], v[28:31], v[196:199], v[164:167]\n"
    "ds_read_b64_tr_b16 v[22:23], v17 offset:6976\n"
    "v_mfma_f32_16x16x32_bf16 v[164:167], v[88:91], v[100:103], v[164:167]\n"
    "ds_read_b64_tr_b16 v[20:21], v17 offset:6208\n"
    "v_mfma_f32_16x16x32_bf16 v[164:167], v[24:27], v[128:131], v[164:167]\n"
    "ds_read_b64_tr_b16 v[142:143], v17 offset:13120\n"
    "v_mfma_f32_16x16x32_bf16 v[164:167], v[120:123], v[40:43], v[164:167]\n"
    "ds_read_b64_tr_b16 v[140:141], v17 offset:12352\n"
    "v_cvt_pk_bf16_f32 v228, v228, v229\n"
    "v_mfma_f32_16x16x32_bf16 v[164:167], v[248:251], v[64:67], v[164:167]\n"
    "ds_read_b64_tr_b16 v[170:171], v2 offset:832\n"
    "v_cvt_pk_bf16_f32 v229, v230, v231\n"
    "v_mfma_f32_16x16x32_bf16 v[164:167], v[116:119], v[172:175], v[164:167]\n"
    "ds_write_b64 v16, v[228:229] offset:0\n"
    "v_mfma_f32_16x16x32_bf16 v[164:167], v[96:99], v[148:151], v[164:167]\n"
    "ds_read_b128 v[232:235], v7 offset:0\n"
    "v_mfma_f32_16x16x32_bf16 v[164:167], v[104:107], v[152:155], v[164:167]\n"
    "ds_read_b64_tr_b16 v[168:169], v2 offset:64\n"
    "v_mfma_f32_16x16x32_bf16 v[164:167], v[80:83], v[184:187], v[164:167]\n"
    "ds_read_b64_tr_b16 v[238:239], v2 offset:6976\n"
    "v_mfma_f32_16x16x32_bf16 v[176:179], v[28:31], v[40:43], v[176:179]\n"
    "ds_read_b64_tr_b16 v[236:237], v2 offset:6208\n"
    "v_mfma_f32_16x16x32_bf16 v[176:179], v[88:91], v[64:67], v[176:179]\n"
    "ds_read_b64_tr_b16 v[62:63], v2 offset:13120\n"
    "v_mfma_f32_16x16x32_bf16 v[176:179], v[24:27], v[172:175], v[176:179]\n"
    "ds_read_b64_tr_b16 v[60:61], v2 offset:12352\n"
    "v_mfma_f32_16x16x32_bf16 v[176:179], v[120:123], v[148:151], v[176:179]\n"
    "ds_read_b64_tr_b16 v[54:55], v9 offset:832\n"
    "v_cvt_pk_bf16_f32 v164, v164, v165\n"
    "v_mfma_f32_16x16x32_bf16 v[176:179], v[248:251], v[152:155], v[176:179]\n"
    "ds_read_b64_tr_b16 v[52:53], v9 offset:64\n"
    "v_cvt_pk_bf16_f32 v165, v166, v167\n"
    "v_mfma_f32_16x16x32_bf16 v[176:179], v[116:119], v[184:187], v[176:179]\n"
    "ds_write_b64 v16, v[164:165] offset:1024\n"
    "v_mfma_f32_16x16x32_bf16 v[176:179], v[96:99], v[212:215], v[176:179]\n"
    "s_waitcnt lgkmcnt(8)\n"
    "buffer_store_dwordx4 v[232:235], v4, s[56:59], s52 offen offset:0 sc1\n"
    "v_mfma_f32_16x16x32_bf16 v[176:179], v[104:107], v[216:219], v[176:179]\n"
    "ds_read_b128 v[144:147], v7 offset:1024\n"
    "v_mfma_f32_16x16x32_bf16 v[176:179], v[80:83], v[220:223], v[176:179]\n"
    "ds_read_b64_tr_b16 v[74:75], v9 offset:6976\n"
    "v_mfma_f32_16x16x32_bf16 v[32:35], v[28:31], v[148:151], v[32:35]\n"
    "ds_read_b64_tr_b16 v[72:73], v9 offset:6208\n"
    "v_mfma_f32_16x16x32_bf16 v[32:35], v[88:91], v[152:155], v[32:35]\n"
    "ds_read_b64_tr_b16 v[70:71], v9 offset:13120\n"
    "v_mfma_f32_16x16x32_bf16 v[32:35], v[24:27], v[184:187], v[32:35]\n"
    "ds_read_b64_tr_b16 v[68:69], v9 offset:12352\n"
    "v_mfma_f32_16x16x32_bf16 v[200:203], v[76:79], v[148:151], v[200:203]\n"
    "ds_read_b64_tr_b16 v[158:159], v18 offset:832\n"
    "v_cvt_pk_bf16_f32 v176, v176, v177\n"
    "v_mfma_f32_16x16x32_bf16 v[200:203], v[84:87], v[152:155], v[200:203]\n"
    "ds_read_b64_tr_b16 v[156:157], v18 offset:64\n"
    "v_cvt_pk_bf16_f32 v177, v178, v179\n"
    "v_mfma_f32_16x16x32_bf16 v[200:203], v[132:135], v[184:187], v[200:203]\n"
    "ds_write_b64 v16, v[176:177] offset:2048\n"
    "v_mfma_f32_16x16x32_bf16 v[200:203], v[124:127], v[212:215], v[200:203]\n"
    "s_waitcnt lgkmcnt(7)\n"
    "buffer_store_dwordx4 v[144:147], v4, s[56:59], s53 offen offset:0 sc1\n"
    "v_mfma_f32_16x16x32_bf16 v[200:203], v[20:23], v[216:219], v[200:203]\n"
    "ds_read_b128 v[228:231], v7 offset:2048\n"
    "v_mfma_f32_16x16x32_bf16 v[200:203], v[140:143], v[220:223], v[200:203]\n"
    "ds_read_b64_tr_b16 v[122:123], v18 offset:6976\n"
    "v_mfma_f32_16x16x32_bf16 v[56:59], v[76:79], v[48:51], v[56:59]\n"
    "ds_read_b64_tr_b16 v[120:121], v18 offset:6208\n"
    "v_mfma_f32_16x16x32_bf16 v[56:59], v[84:87], v[136:139], v[56:59]\n"
    "ds_read_b64_tr_b16 v[250:251], v18 offset:13120\n"
    "v_mfma_f32_16x16x32_bf16 v[56:59], v[132:135], v[44:47], v[56:59]\n"
    "ds_read_b64_tr_b16 v[248:249], v18 offset:12352\n"
    "v_mfma_f32_16x16x32_bf16 v[56:59], v[124:127], v[196:199], v[56:59]\n"
    "ds_write_b64 v13, v[34:35] offset:1032\n"
    "v_mfma_f32_16x16x32_bf16 v[56:59], v[20:23], v[100:103], v[56:59]\n"
    "ds_write_b64 v13, v[32:33] offset:1024\n"
    "v_mfma_f32_16x16x32_bf16 v[56:59], v[140:143], v[128:131], v[56:59]\n"
    "ds_read_b64_tr_b16 v[118:119], v3 offset:832\n"
    "v_mfma_f32_16x16x32_bf16 v[56:59], v[168:171], v[40:43], v[56:59]\n"
    "s_waitcnt lgkmcnt(7)\n"
    "buffer_store_dwordx4 v[228:231], v4, s[56:59], s54 offen offset:0 sc1\n"
    "v_mfma_f32_16x16x32_bf16 v[56:59], v[236:239], v[64:67], v[56:59]\n"
    "LPT2:\n"
    "s_cbranch_execz LCS\n"
    "ds_read_b64_tr_b16 v[116:117], v3 offset:64\n"
    "v_mfma_f32_16x16x32_bf16 v[56:59], v[60:63], v[172:175], v[56:59]\n"
    "ds_write_b32 v5, v12 offset:0\n"
    "v_mfma_f32_16x16x32_bf16 v[36:39], v[52:55], v[148:151], v[36:39]\n"
    "ds_read_b64_tr_b16 v[166:167], v3 offset:6976\n"
    "v_mfma_f32_16x16x32_bf16 v[36:39], v[72:75], v[152:155], v[36:39]\n"
    "ds_read_b64_tr_b16 v[164:165], v3 offset:6208\n"
    "v_mfma_f32_16x16x32_bf16 v[36:39], v[68:71], v[184:187], v[36:39]\n"
    "ds_read_b64_tr_b16 v[98:99], v3 offset:13120\n"
    "v_mfma_f32_16x16x32_bf16 v[224:227], v[52:55], v[48:51], v[224:227]\n"
    "ds_read_b64_tr_b16 v[96:97], v3 offset:12352\n"
    "v_cvt_pk_bf16_f32 v56, v56, v57\n"
    "v_mfma_f32_16x16x32_bf16 v[224:227], v[72:75], v[136:139], v[224:227]\n"
    "ds_write_b64 v13, v[202:203] offset:2056\n"
    "v_mfma_f32_16x16x32_bf16 v[224:227], v[68:71], v[44:47], v[224:227]\n"
    "ds_write_b64 v13, v[200:201] offset:2048\n"
    "v_mfma_f32_16x16x32_bf16 v[224:227], v[156:159], v[196:199], v[224:227]\n"
    "v_cvt_pk_bf16_f32 v57, v58, v59\n"
    "s_waitcnt lgkmcnt(8)\n"
    "v_mfma_f32_16x16x32_bf16 v[224:227], v[120:123], v[100:103], v[224:227]\n"
    "ds_write_b64 v14, v[56:57] offset:0\n"
    "v_mfma_f32_16x16x32_bf16 v[224:227], v[248:251], v[128:131], v[224:227]\n"
    "s_waitcnt lgkmcnt(7)\n"
    "v_mfma_f32_16x16x32_bf16 v[224:227], v[116:119], v[40:43], v[224:227]\n"
    "s_waitcnt lgkmcnt(5)\n"
    "v_mfma_f32_16x16x32_bf16 v[224:227], v[164:167], v[64:67], v[224:227]\n"
    "s_waitcnt lgkmcnt(3)\n"
    "v_mfma_f32_16x16x32_bf16 v[224:227], v[96:99], v[172:175], v[224:227]\n"
    "ds_write_b64 v13, v[38:39] offset:3080\n"
    "v_mfma_f32_16x16x32_bf16 v[188:191], v[76:79], v[196:199], v[188:191]\n"
    "ds_write_b64 v13, v[36:37] offset:3072\n"
    "v_mfma_f32_16x16x32_bf16 v[188:191], v[84:87], v[100:103], v[188:191]\n"
    "ds_write_b32 v5, v12 offset:16\n"
    "v_mfma_f32_16x16x32_bf16 v[188:191], v[132:135], v[128:131], v[188:191]\n"
    "v_mfma_f32_16x16x32_bf16 v[188:191], v[124:127], v[40:43], v[188:191]\n"
    "s_nop 0\n"
    "v_cvt_pk_bf16_f32 v224, v224, v225\n"
    "v_mfma_f32_16x16x32_bf16 v[188:191], v[20:23], v[64:67], v[188:191]\n"
    "v_cvt_pk_bf16_f32 v225, v226, v227\n"
    "ds_write_b64 v16, v[224:225] offset:0\n"
    "v_mfma_f32_16x16x32_bf16 v[188:191], v[140:143], v[172:175], v[188:191]\n"
    "ds_read_b128 v[232:235], v7 offset:0\n"
    "v_mfma_f32_16x16x32_bf16 v[188:191], v[168:171], v[148:151], v[188:191]\n"
    "ds_read_b64_tr_b16 v[106:107], v8 offset:896\n"
    "v_mfma_f32_16x16x32_bf16 v[188:191], v[236:239], v[152:155], v[188:191]\n"
    "ds_read_b64_tr_b16 v[104:105], v8 offset:128\n"
    "v_mfma_f32_16x16x32_bf16 v[188:191], v[60:63], v[184:187], v[188:191]\n"
    "ds_read_b64_tr_b16 v[82:83], v8 offset:7040\n"
    "v_mfma_f32_16x16x32_bf16 v[160:163], v[52:55], v[196:199], v[160:163]\n"
    "ds_read_b64_tr_b16 v[80:81], v8 offset:6272\n"
    "v_mfma_f32_16x16x32_bf16 v[160:163], v[72:75], v[100:103], v[160:163]\n"
    "ds_read_b64_tr_b16 v[30:31], v8 offset:13184\n"
    "v_mfma_f32_16x16x32_bf16 v[160:163], v[68:71], v[128:131], v[160:163]\n"
    "ds_read_b64_tr_b16 v[28:29], v8 offset:12416\n"
    "v_mfma_f32_16x16x32_bf16 v[160:163], v[156:159], v[40:43], v[160:163]\n"
    "ds_read_b64_tr_b16 v[90:91], v17 offset:896\n"
    "v_cvt_pk_bf16_f32 v188, v188, v189\n"
    "v_mfma_f32_16x16x32_bf16 v[160:163], v[120:123], v[64:67], v[160:163]\n"
    "ds_read_b64_tr_b16 v[88:89], v17 offset:128\n"
    "s_waitcnt lgkmcnt(8)\n"
    "buffer_store_dwordx4 v[232:235], v4, s[56:59], s52 offen offset:64 sc1\n"
    "v_mfma_f32_16x16x32_bf16 v[160:163], v[248:251], v[172:175], v[160:163]\n"
    "ds_read_b64_tr_b16 v[26:27], v17 offset:7040\n"
    "v_cvt_pk_bf16_f32 v189, v190, v191\n"
    "v_mfma_f32_16x16x32_bf16 v[160:163], v[116:119], v[148:151], v[160:163]\n"
    "ds_write_b64 v14, v[188:189] offset:1024\n"
    "v_mfma_f32_16x16x32_bf16 v[160:163], v[164:167], v[152:155], v[160:163]\n"
    "ds_read_b64_tr_b16 v[24:25], v17 offset:6272\n"
    "v_mfma_f32_16x16x32_bf16 v[160:163], v[96:99], v[184:187], v[160:163]\n"
    "ds_read_b64_tr_b16 v[178:179], v17 offset:13184\n"
    "v_mfma_f32_16x16x32_bf16 v[192:195], v[76:79], v[40:43], v[192:195]\n"
    "ds_read_b64_tr_b16 v[176:177], v17 offset:12416\n"
    "v_mfma_f32_16x16x32_bf16 v[192:195], v[84:87], v[64:67], v[192:195]\n"
    "ds_read_b64_tr_b16 v[146:147], v2 offset:896\n"
    "v_mfma_f32_16x16x32_bf16 v[192:195], v[132:135], v[172:175], v[192:195]\n"
    "ds_read_b64_tr_b16 v[144:145], v2 offset:128\n"
    "v_mfma_f32_16x16x32_bf16 v[192:195], v[124:127], v[148:151], v[192:195]\n"
    "ds_read_b64_tr_b16 v[34:35], v2 offset:7040\n"
    "v_cvt_pk_bf16_f32 v160, v160, v161\n"
    "v_mfma_f32_16x16x32_bf16 v[192:195], v[20:23], v[152:155], v[192:195]\n"
    "ds_read_b64_tr_b16 v[32:33], v2 offset:6272\n"
    "v_cvt_pk_bf16_f32 v161, v162, v163\n"
    "v_mfma_f32_16x16x32_bf16 v[192:195], v[140:143], v[184:187], v[192:195]\n"
    "ds_write_b64 v16, v[160:161] offset:1024\n"
    "v_mfma_f32_16x16x32_bf16 v[192:195], v[168:171], v[212:215], v[192:195]\n"
    "ds_read_b128 v[228:231], v7 offset:1024\n"
    "v_mfma_f32_16x16x32_bf16 v[192:195], v[236:239], v[216:219], v[192:195]\n"
    "ds_read_b64_tr_b16 v[202:203], v2 offset:13184\n"
    "v_mfma_f32_16x16x32_bf16 v[192:195], v[60:63], v[220:223], v[192:195]\n"
    "ds_read_b64_tr_b16 v[200:201], v2 offset:12416\n"
    "v_mfma_f32_16x16x32_bf16 v[204:207], v[52:55], v[40:43], v[204:207]\n"
    "ds_read_b64_tr_b16 v[58:59], v9 offset:896\n"
    "v_mfma_f32_16x16x32_bf16 v[204:207], v[72:75], v[64:67], v[204:207]\n"
    "ds_read_b64_tr_b16 v[56:57], v9 offset:128\n"
    "v_mfma_f32_16x16x32_bf16 v[204:207], v[68:71], v[172:175], v[204:207]\n"
    "ds_read_b64_tr_b16 v[38:39], v9 offset:7040\n"
    "v_mfma_f32_16x16x32_bf16 v[204:207], v[156:159], v[148:151], v[204:207]\n"
    "ds_read_b64_tr_b16 v[36:37], v9 offset:6272\n"
    "v_cvt_pk_bf16_f32 v192, v192, v193\n"
    "v_mfma_f32_16x16x32_bf16 v[204:207], v[120:123], v[152:155], v[204:207]\n"
    "ds_read_b64_tr_b16 v[226:227], v9 offset:13184\n"
    "v_cvt_pk_bf16_f32 v193, v194, v195\n"
    "v_mfma_f32_16x16x32_bf16 v[204:207], v[248:251], v[184:187], v[204:207]\n"
    "ds_write_b64 v14, v[192:193] offset:2048\n"
    "v_mfma_f32_16x16x32_bf16 v[204:207], v[116:119], v[212:215], v[204:207]\n"
    "s_waitcnt lgkmcnt(8)\n"
    "buffer_store_dwordx4 v[228:231], v4, s[56:59], s53 offen offset:64 sc1\n"
    "v_mfma_f32_16x16x32_bf16 v[204:207], v[164:167], v[216:219], v[204:207]\n"
    "ds_read_b64_tr_b16 v[224:225], v9 offset:12416\n"
    "v_mfma_f32_16x16x32_bf16 v[204:207], v[96:99], v[220:223], v[204:207]\n"
    "ds_read_b64_tr_b16 v[234:235], v18 offset:896\n"
    "v_mfma_f32_16x16x32_bf16 v[240:243], v[104:107], v[148:151], v[240:243]\n"
    "ds_read_b64_tr_b16 v[232:233], v18 offset:128\n"
    "v_mfma_f32_16x16x32_bf16 v[240:243], v[80:83], v[152:155], v[240:243]\n"
    "ds_read_b64_tr_b16 v[190:191], v18 offset:7040\n"
    "v_mfma_f32_16x16x32_bf16 v[240:243], v[28:31], v[184:187], v[240:243]\n"
    "ds_read_b64_tr_b16 v[188:189], v18 offset:6272\n"
    "v_mfma_f32_16x16x32_bf16 v[240:243], v[88:91], v[212:215], v[240:243]\n"
    "ds_read_b64_tr_b16 v[78:79], v18 offset:13184\n"
    "v_cvt_pk_bf16_f32 v204, v204, v205\n"
    "v_mfma_f32_16x16x32_bf16 v[240:243], v[24:27], v[216:219], v[240:243]\n"
    "ds_read_b64_tr_b16 v[76:77], v18 offset:12416\n"
    "v_cvt_pk_bf16_f32 v205, v206, v207\n"
    "v_mfma_f32_16x16x32_bf16 v[240:243], v[176:179], v[220:223], v[240:243]\n"
    "ds_write_b64 v16, v[204:205] offset:2048\n"
    "v_mfma_f32_16x16x32_bf16 v[108:111], v[104:107], v[48:51], v[108:111]\n"
    "ds_read_b128 v[84:87], v7 offset:2048\n"
    "s_nop 0\n"
    "v_mfma_f32_16x16x32_bf16 v[108:111], v[80:83], v[136:139], v[108:111]\n"
    "ds_read_b64_tr_b16 v[134:135], v3 offset:896\n"
    "s_nop 0\n"
    "v_mfma_f32_16x16x32_bf16 v[108:111], v[28:31], v[44:47], v[108:111]\n"
    "ds_read_b64_tr_b16 v[132:133], v3 offset:128\n"
    "v_mfma_f32_16x16x32_bf16 v[108:111], v[88:91], v[196:199], v[108:111]\n"
    "ds_read_b64_tr_b16 v[126:127], v3 offset:7040\n"
    "v_mfma_f32_16x16x32_bf16 v[108:111], v[24:27], v[100:103], v[108:111]\n"
    "ds_read_b64_tr_b16 v[124:125], v3 offset:6272\n"
    "v_mfma_f32_16x16x32_bf16 v[108:111], v[176:179], v[128:131], v[108:111]\n"
    "ds_read_b64_tr_b16 v[22:23], v3 offset:13184\n"
    "v_mfma_f32_16x16x32_bf16 v[108:111], v[144:147], v[40:43], v[108:111]\n"
    "ds_read_b64_tr_b16 v[20:21], v3 offset:12416\n"
    "v_mfma_f32_16x16x32_bf16 v[108:111], v[32:35], v[64:67], v[108:111]\n"
    "s_waitcnt lgkmcnt(9)\n"
    "v_mfma_f32_16x16x32_bf16 v[108:111], v[200:203], v[172:175], v[108:111]\n"
    "ds_write_b64 v13, v[242:243] offset:4104\n"
    "v_mfma_f32_16x16x32_bf16 v[112:115], v[56:59], v[148:151], v[112:115]\n"
    "s_waitcnt lgkmcnt(7)\n"
    "buffer_store_dwordx4 v[84:87], v4, s[56:59], s54 offen offset:64 sc1\n"
    "v_mfma_f32_16x16x32_bf16 v[112:115], v[36:39], v[152:155], v[112:115]\n"
    "ds_write_b64 v13, v[240:241] offset:4096\n"
    "v_mfma_f32_16x16x32_bf16 v[112:115], v[224:227], v[184:187], v[112:115]\n"
    "s_nop 0\n"
    "v_cvt_pk_bf16_f32 v108, v108, v109\n"
    "v_mfma_f32_16x16x32_bf16 v[92:95], v[56:59], v[48:51], v[92:95]\n"
    "v_cvt_pk_bf16_f32 v109, v110, v111\n"
    "s_nop 0\n"
    "v_mfma_f32_16x16x32_bf16 v[92:95], v[36:39], v[136:139], v[92:95]\n"
    "ds_write_b64 v14, v[108:109] offset:0\n"
    "v_mfma_f32_16x16x32_bf16 v[92:95], v[224:227], v[44:47], v[92:95]\n"
    "ds_write_b64 v13, v[114:115] offset:5128\n"
    "v_mfma_f32_16x16x32_bf16 v[92:95], v[232:235], v[196:199], v[92:95]\n"
    "ds_write_b64 v13, v[112:113] offset:5120\n"
    "v_mfma_f32_16x16x32_bf16 v[92:95], v[188:191], v[100:103], v[92:95]\n"
    "ds_write_b32 v5, v12 offset:32\n"
    "v_mfma_f32_16x16x32_bf16 v[92:95], v[76:79], v[128:131], v[92:95]\n"
    "s_waitcnt lgkmcnt(5)\n"
    "v_mfma_f32_16x16x32_bf16 v[92:95], v[132:135], v[40:43], v[92:95]\n"
    "v_mfma_f32_16x16x32_bf16 v[92:95], v[124:127], v[64:67], v[92:95]\n"
    "v_mfma_f32_16x16x32_bf16 v[92:95], v[20:23], v[172:175], v[92:95]\n"
    "s_nop 1\n"
    "v_mfma_f32_16x16x32_bf16 v[208:211], v[104:107], v[196:199], v[208:211]\n"
    "s_nop 1\n"
    "v_mfma_f32_16x16x32_bf16 v[208:211], v[80:83], v[100:103], v[208:211]\n"
    "s_nop 1\n"
    "v_mfma_f32_16x16x32_bf16 v[208:211], v[28:31], v[128:131], v[208:211]\n"
    "v_cvt_pk_bf16_f32 v92, v92, v93\n"
    "v_cvt_pk_bf16_f32 v93, v94, v95\n"
    "v_mfma_f32_16x16x32_bf16 v[208:211], v[88:91], v[40:43], v[208:211]\n"
    "ds_write_b64 v16, v[92:93] offset:0\n"
    "v_mfma_f32_16x16x32_bf16 v[208:211], v[24:27], v[64:67], v[208:211]\n"
    "ds_read_b128 v[140:143], v7 offset:0\n"
    "v_mfma_f32_16x16x32_bf16 v[208:211], v[176:179], v[172:175], v[208:211]\n"
    "v_mfma_f32_16x16x32_bf16 v[208:211], v[144:147], v[148:151], v[208:211]\n"
    "v_mfma_f32_16x16x32_bf16 v[208:211], v[32:35], v[152:155], v[208:211]\n"
    "v_mfma_f32_16x16x32_bf16 v[208:211], v[200:203], v[184:187], v[208:211]\n"
    "s_nop 1\n"
    "v_mfma_f32_16x16x32_bf16 v[244:247], v[56:59], v[196:199], v[244:247]\n"
    "s_nop 1\n"
    "v_mfma_f32_16x16x32_bf16 v[244:247], v[36:39], v[100:103], v[244:247]\n"
    "s_nop 1\n"
    "v_mfma_f32_16x16x32_bf16 v[244:247], v[224:227], v[128:131], v[244:247]\n"
    "v_cvt_pk_bf16_f32 v208, v208, v209\n"
    "v_cvt_pk_bf16_f32 v209, v210, v211\n"
    "v_mfma_f32_16x16x32_bf16 v[244:247], v[232:235], v[40:43], v[244:247]\n"
    "ds_write_b64 v14, v[208:209] offset:1024\n"
    "v_mfma_f32_16x16x32_bf16 v[244:247], v[188:191], v[64:67], v[244:247]\n"
    "s_waitcnt lgkmcnt(1)\n"
    "buffer_store_dwordx4 v[140:143], v4, s[56:59], s52 offen offset:128 sc1\n"
    "v_mfma_f32_16x16x32_bf16 v[244:247], v[76:79], v[172:175], v[244:247]\n"
    "v_mfma_f32_16x16x32_bf16 v[244:247], v[132:135], v[148:151], v[244:247]\n"
    "v_mfma_f32_16x16x32_bf16 v[244:247], v[124:127], v[152:155], v[244:247]\n"
    "v_mfma_f32_16x16x32_bf16 v[244:247], v[20:23], v[184:187], v[244:247]\n"
    "s_nop 1\n"
    "v_mfma_f32_16x16x32_bf16 v[180:183], v[104:107], v[40:43], v[180:183]\n"
    "s_nop 1\n"
    "v_mfma_f32_16x16x32_bf16 v[180:183], v[80:83], v[64:67], v[180:183]\n"
    "s_nop 1\n"
    "v_mfma_f32_16x16x32_bf16 v[180:183], v[28:31], v[172:175], v[180:183]\n"
    "v_cvt_pk_bf16_f32 v244, v244, v245\n"
    "v_cvt_pk_bf16_f32 v245, v246, v247\n"
    "v_mfma_f32_16x16x32_bf16 v[180:183], v[88:91], v[148:151], v[180:183]\n"
    "ds_write_b64 v16, v[244:245] offset:1024\n"
    "v_mfma_f32_16x16x32_bf16 v[180:183], v[24:27], v[152:155], v[180:183]\n"
    "ds_read_b128 v[160:163], v7 offset:1024\n"
    "v_mfma_f32_16x16x32_bf16 v[180:183], v[176:179], v[184:187], v[180:183]\n"
    "v_mfma_f32_16x16x32_bf16 v[180:183], v[144:147], v[212:215], v[180:183]\n"
    "v_mfma_f32_16x16x32_bf16 v[180:183], v[32:35], v[216:219], v[180:183]\n"
    "v_mfma_f32_16x16x32_bf16 v[180:183], v[200:203], v[220:223], v[180:183]\n"
    "s_nop 1\n"
    "v_mfma_f32_16x16x32_bf16 v[252:255], v[56:59], v[40:43], v[252:255]\n"
    "s_nop 1\n"
    "v_mfma_f32_16x16x32_bf16 v[252:255], v[36:39], v[64:67], v[252:255]\n"
    "s_nop 1\n"
    "v_mfma_f32_16x16x32_bf16 v[252:255], v[224:227], v[172:175], v[252:255]\n"
    "v_cvt_pk_bf16_f32 v180, v180, v181\n"
    "v_cvt_pk_bf16_f32 v181, v182, v183\n"
    "v_mfma_f32_16x16x32_bf16 v[252:255], v[232:235], v[148:151], v[252:255]\n"
    "ds_write_b64 v14, v[180:181] offset:2048\n"
    "v_mfma_f32_16x16x32_bf16 v[252:255], v[188:191], v[152:155], v[252:255]\n"
    "s_waitcnt lgkmcnt(1)\n"
    "buffer_store_dwordx4 v[160:163], v4, s[56:59], s53 offen offset:128 sc1\n"
    "v_mfma_f32_16x16x32_bf16 v[252:255], v[76:79], v[184:187], v[252:255]\n"
    "v_mfma_f32_16x16x32_bf16 v[252:255], v[132:135], v[212:215], v[252:255]\n"
    "v_mfma_f32_16x16x32_bf16 v[252:255], v[124:127], v[216:219], v[252:255]\n"
    "v_mfma_f32_16x16x32_bf16 v[252:255], v[20:23], v[220:223], v[252:255]\n"
    "s_nop 7\n"
    "v_cvt_pk_bf16_f32 v252, v252, v253\n"
    "v_cvt_pk_bf16_f32 v253, v254, v255\n"
    "ds_write_b64 v16, v[252:253] offset:2048\n"
    "ds_read_b128 v[168:171], v7 offset:2048\n"
    "s_waitcnt lgkmcnt(0)\n"
    "buffer_store_dwordx4 v[168:171], v4, s[56:59], s54 offen offset:128 sc1\n"
    "s_endpgm\n"
    "LF_16:\n"
    "LP_25:\n"
    "ds_read_b32 v15, v12 offset:24\n"
    "s_waitcnt lgkmcnt(0)\n"
    "v_cmp_ne_u32 vcc, 0, v15\n"
    "s_sub_u32 s39, s39, 1\n"
    "s_cbranch_scc1 LDEAD\n"
    "s_cbranch_vccnz LP_25\n"
    "s_branch LF_16b\n"
    "LH0:\n"
    "s_branch LH1\n"
    "LF_17:\n"
    "LP_26:\n"
    "ds_read_b32 v1, v12 offset:28\n"
    "s_waitcnt lgkmcnt(0)\n"
    "v_cmp_ne_u32 vcc, 0, v1\n"
    "s_sub_u32 s39, s39, 1\n"
    "s_cbranch_scc1 LDEAD\n"
    "s_cbranch_vccnz LP_26\n"
    "s_branch LF_17b\n"
    "LF_18:\n"
    "LP_27:\n"
    "ds_read_b32 v15, v12 offset:32\n"
    "s_waitcnt lgkmcnt(0)\n"
    "v_cmp_ne_u32 vcc, 0, v15\n"
    "s_sub_u32 s39, s39, 1\n"
    "s_cbranch_scc1 LDEAD\n"
    "s_cbranch_vccnz LP_27\n"
    "s_branch LF_18b\n"
    "LH1:\n"
    "s_branch LH2\n"
    "LF_19:\n"
    "LP_28:\n"
    "ds_read_b32 v1, v12 offset:36\n"
    "s_waitcnt lgkmcnt(0)\n"
    "v_cmp_ne_u32 vcc, 0, v1\n"
    "s_sub_u32 s39, s39, 1\n"
    "s_cbranch_scc1 LDEAD\n"
    "s_cbranch_vccnz LP_28\n"
    "s_branch LF_19b\n"
    "LF_20:\n"
    "LP_29:\n"
    "ds_read_b32 v15, v12 offset:40\n"
    "s_waitcnt lgkmcnt(0)\n"
    "v_cmp_ne_u32 vcc, 0, v15\n"
    "s_sub_u32 s39, s39, 1\n"
    "s_cbranch_scc1 LDEAD\n"
    "s_cbranch_vccnz LP_29\n"
    "s_branch LF_20b\n"
    "LH2:\n"
    "s_branch LH3\n"
    "LF_21:\n"
    "LP_30:\n"
    "ds_read_b32 v1, v12 offset:44\n"
    "s_waitcnt lgkmcnt(0)\n"
    "v_cmp_ne_u32 vcc, 0, v1\n"
    "s_sub_u32 s39, s39, 1\n"
    "s_cbranch_scc1 LDEAD\n"
    "s_cbranch_vccnz LP_30\n"
    "s_branch LF_21b\n"
    "LF_22:\n"
    "LP_31:\n"
    "ds_read_b32 v15, v12 offset:48\n"
    "s_waitcnt lgkmcnt(0)\n"
    "v_cmp_ne_u32 vcc, 0, v15\n"
    "s_sub_u32 s39, s39, 1\n"
    "s_cbranch_scc1 LDEAD\n"
    "s_cbranch_vccnz LP_31\n"
    "s_branch LF_22b\n"
    "LH3:\n"
    "s_branch LH4\n"
    "LF_23:\n"
    "LP_32:\n"
    "ds_read_b32 v1, v12 offset:56\n"
    "s_waitcnt lgkmcnt(0)\n"
    "v_cmp_ne_u32 vcc, 0, v1\n"
    "s_sub_u32 s39, s39, 1\n"
    "s_cbranch_scc1 LDEAD\n"
    "s_cbranch_vccnz LP_32\n"
    "s_branch LF_23b\n"
    "LF_24:\n"
    "LP_33:\n"
    "ds_read_b32 v15, v12 offset:64\n"
    "s_waitcnt lgkmcnt(0)\n"
    "v_cmp_ne_u32 vcc, 0, v15\n"
    "s_sub_u32 s39, s39, 1\n"
    "s_cbranch_scc1 LDEAD\n"
    "s_cbranch_vccnz LP_33\n"
    "s_branch LF_24b\n"
    "LH4:\n"
    "s_branch LCS\n"
    "LDEAD:\n"
    "s_endpgm\n"
    "LEND:\n"
    ".fill 1024, 4, 0xbf800000\n"
    :: "s"(__builtin_amdgcn_kernarg_segment_ptr()), "s"(__builtin_amdgcn_workgroup_id_x()),
       "v"(__builtin_amdgcn_workitem_id_x()), "s"(ldsb)
    : "v1","v2","v3","v4","v5","v6","v7","v8","v9","v10","v11","v12","v13","v14","v15","v16","v17","v18","v19","v20","v21","v22","v23","v24","v25","v26","v27","v28","v29","v30","v31","v32","v33","v34","v35","v36","v37","v38","v39","v40","v41","v42","v43","v44","v45","v46","v47","v48","v49","v50","v51","v52","v53","v54","v55","v56","v57","v58","v59","v60","v61","v62","v63","v64","v65","v66","v67","v68","v69","v70","v71","v72","v73","v74","v75","v76","v77","v78","v79","v80","v81","v82","v83","v84","v85","v86","v87","v88","v89","v90","v91","v92","v93","v94","v95","v96","v97","v98","v99","v100","v101","v102","v103","v104","v105","v106","v107","v108","v109","v110","v111","v112","v113","v114","v115","v116","v117","v118","v119","v120","v121","v122","v123","v124","v125","v126","v127","v128","v129","v130","v131","v132","v133","v134","v135","v136","v137","v138","v139","v140","v141","v142","v143","v144","v145","v146","v147","v148","v149","v150","v151","v152","v153","v154","v155","v156","v157","v158","v159","v160","v161","v162","v163","v164","v165","v166","v167","v168","v169","v170","v171","v172","v173","v174","v175","v176","v177","v178","v179","v180","v181","v182","v183","v184","v185","v186","v187","v188","v189","v190","v191","v192","v193","v194","v195","v196","v197","v198","v199","v200","v201","v202","v203","v204","v205","v206","v207","v208","v209","v210","v211","v212","v213","v214","v215","v216","v217","v218","v219","v220","v221","v222","v223","v224","v225","v226","v227","v228","v229","v230","v231","v232","v233","v234","v235","v236","v237","v238","v239","v240","v241","v242","v243","v244","v245","v246","v247","v248","v249","v250","v251","v252","v253","v254","v255","s4","s5","s6","s7","s8","s9","s10","s11","s12","s13","s14","s15","s16","s17","s18","s19","s20","s21","s22","s23","s24","s25","s26","s27","s28","s29","s30","s31","s34","s35","s36","s37","s38","s39","s40","s41","s42","s43","s44","s45","s46","s47","s48","s49","s50","s51","s52","s53","s54","s55","s56","s57","s58","s59","s60","s61","s62","s63","s64","s65","s66","s67","s68","s69","s70","s71","s72","s73","s74","s75","s76","s77","s78","s79","s80","s81","s82","s83","s84","s85","s86","s87","s88","s89","s90","s91","s92","s93","s94","s95","s96","s97","s98","s99", "vcc", "scc", "memory");
}

extern "C" void kernel_launch(void* const* d_in, const int* in_sizes, int n_in, void* d_out, int out_size,
                              void* d_ws, size_t ws_size, hipStream_t stream) {
  const unsigned F = 0x00020000u;
  fpn_kernel<<<256, 320, 0, stream>>>(d_in[0], 224u*224u*192u, F, d_in[1], 112u*112u*192u, F,
                                      d_in[2], 96u*192u, F, d_in[3], 9u*96u*192u, F,
                                      d_out, 224u*224u*192u, F);
}
```
